```python
import torch

def _softmax(x, axis=-1):
    return torch.softmax(x, dim=axis)
def _gelu(x):
    return torch.nn.functional.gelu(x, approximate="tanh")
def _silu(x):
    return torch.nn.functional.silu(x)
def _sigmoid(x):
    return torch.sigmoid(x)
def _layernorm(x, axis=-1, eps=1e-5):
    m = x.mean(dim=axis, keepdim=True)
    v = x.var(dim=axis, keepdim=True, unbiased=False)
    return (x - m) / torch.sqrt(v + eps)
def _scatter_add(tgt, idx, src, axis=0):
    return tgt.clone().index_add_(axis, idx, src)
def _segment_sum(src, seg, n):
    out = torch.zeros((n,) + src.shape[1:], dtype=src.dtype)
    return out.index_add_(0, seg, src)
def _pad(x, pw):
    flat = tuple(v for pair in reversed(pw) for v in pair)
    return torch.nn.functional.pad(x, flat)

kernel_name = 'parallel_sum__mha_block_cached__geglu_ffn__decode'

def setup_inputs():

    _w = (lambda a: (lambda t: t.to(torch.bfloat16) if t.is_floating_point() else t)(torch.as_tensor(a)))
    def _A_setup():
        B, Sq, D = 1, 1, 576
        H, Sk, Dh = 32, 16384, 128
        return {
            "x": _w((lambda *s: torch.randn(*s, dtype=torch.bfloat16) * 0.05)(B, Sq, D)),
            "k_cache": _w((lambda *s: torch.randn(*s, dtype=torch.bfloat16) * 3.2)(B, H, Sk, Dh)),
            "v_cache": _w((lambda *s: torch.randn(*s, dtype=torch.bfloat16) * 1.6)(B, H, Sk, Dh)),
            "wq": _w((lambda *s: torch.randn(*s, dtype=torch.bfloat16) * 3.2)(D, H * Dh) * (D ** -0.5)),
            "wo": _w((lambda *s: torch.randn(*s, dtype=torch.bfloat16) * 1.6)(H * Dh, D) * ((H * Dh) ** -0.5)),
        }
    _A_in = _A_setup()

    def _B_setup():
        B, S, D, F = 1, 1, 576, 4608
        return {
            "x": _w((lambda *s: torch.randn(*s, dtype=torch.bfloat16) * 0.05)(B, S, D)),
            "w_gate": _w((lambda *s: torch.randn(*s, dtype=torch.bfloat16) * 0.05)(D, F)),
            "w_up": _w((lambda *s: torch.randn(*s, dtype=torch.bfloat16) * 0.05)(D, F)),
            "w_down": _w((lambda *s: torch.randn(*s, dtype=torch.bfloat16) * 0.05)(F, D)),
        }
    _B_in = _B_setup()

    return {
        "A_x": _A_in["x"],
        "A_k_cache": _A_in["k_cache"],
        "A_v_cache": _A_in["v_cache"],
        "A_wq": _A_in["wq"],
        "A_wo": _A_in["wo"],
        "B_x": _B_in["x"],
        "B_w_gate": _B_in["w_gate"],
        "B_w_up": _B_in["w_up"],
        "B_w_down": _B_in["w_down"],
    }

def reference(A_x, A_k_cache, A_v_cache, A_wq, A_wo, B_x, B_w_gate, B_w_up, B_w_down):
    def _A_ref(x, k_cache, v_cache, wq, wo):
        B, Sq, D = x.shape
        H, Dh = 32, 128
        q = (lambda x, *d: x.permute(*d))((x @ wq).reshape(B, Sq, H, Dh), 0, 2, 1, 3)
        scores = torch.einsum("bhqd,bhkd->bhqk", q, k_cache) * (Dh ** -0.5)
        p = _softmax(scores, axis=-1)
        o = torch.einsum("bhqk,bhkd->bhqd", p, v_cache)
        return (lambda x, *d: x.permute(*d))(o, 0, 2, 1, 3).reshape(B, Sq, H * Dh) @ wo

    def _B_ref(x, w_gate, w_up, w_down):
        return (_gelu(x @ w_gate) * (x @ w_up)) @ w_down

    x = A_x
    xn = _layernorm(x, axis=-1)
    return x + _A_ref(xn, A_k_cache, A_v_cache, A_wq, A_wo) + _B_ref(xn, B_w_gate, B_w_up, B_w_down)
```

```cpp
#include <hip/hip_runtime.h>
#include <hip/hip_bf16.h>
#include <stdint.h>

namespace {

typedef unsigned u32x4 __attribute__((ext_vector_type(4)));
typedef unsigned u32x2 __attribute__((ext_vector_type(2)));
typedef float f32x4 __attribute__((ext_vector_type(4)));
typedef float f32x2 __attribute__((ext_vector_type(2)));
typedef __bf16 bf16x2_t __attribute__((ext_vector_type(2)));
typedef unsigned short u16;
typedef unsigned long long u64;

constexpr int D = 576, NH = 32, SK = 16384, DH = 128, FF = 4608;
constexpr int NWG = 256, NTHR = 512;
constexpr int NSPLIT = 8, TOK_WG = SK / NSPLIT;
constexpr int NTILE = 8;
constexpr int NRED = 144;
constexpr float L2E = 1.4426950408889634f;
constexpr float SCALE = 0.08838834764831845f;

constexpr unsigned WS_E = 0;
constexpr unsigned WS_IN1 = 4096;
constexpr unsigned WS_IN2 = WS_IN1 + 32 * 2 * 8 * 1024;
constexpr unsigned WS_END = WS_IN2 + NRED * 256 * 32;
constexpr unsigned SPIN_CAP = 1u << 18;

constexpr int L_XN = 0;
constexpr int L_XR = L_XN + 576;
constexpr int L_QP = L_XR + 576;
constexpr int L_MRG = L_QP + 8 * 128;
constexpr int L_REC = L_MRG + 8 * 132;
constexpr int L_GU = L_REC + 8 * 68;
constexpr int L_RED = L_GU + 8 * 48;
constexpr int L_FIN = L_RED + 8 * 576;
constexpr int L_TOTAL = L_FIN + 32;

__device__ __forceinline__ float bflo(unsigned u) { return __uint_as_float(u << 16); }
__device__ __forceinline__ float bfhi(unsigned u) { return __uint_as_float(u & 0xffff0000u); }
__device__ __forceinline__ float rbf(float x) { return (float)(__bf16)x; }
__device__ __forceinline__ unsigned packbf(float lo, float hi) {
  bf16x2_t v; v[0] = (__bf16)lo; v[1] = (__bf16)hi; return __builtin_bit_cast(unsigned, v);
}
__device__ __forceinline__ float dot2(unsigned a, unsigned b, float acc) {
  return __builtin_amdgcn_fdot2_f32_bf16(__builtin_bit_cast(bf16x2_t, a), __builtin_bit_cast(bf16x2_t, b), acc, false);
}
template <int CTRL> __device__ __forceinline__ float dpp(float x) {
  return __builtin_bit_cast(float, __builtin_amdgcn_mov_dpp(__builtin_bit_cast(int, x), CTRL, 0xf, 0xf, true));
}
constexpr int XOR1 = 0xB1, XOR2 = 0x4E, XOR7 = 0x141, XOR8 = 0x128, ROR2 = 0x122, ROR4 = 0x124;
__device__ __forceinline__ float xrow_max(float x) {
  auto s = __builtin_amdgcn_permlane16_swap(__float_as_uint(x), __float_as_uint(x), false, false);
  x = fmaxf(__uint_as_float(s[0]), __uint_as_float(s[1]));
  auto t = __builtin_amdgcn_permlane32_swap(__float_as_uint(x), __float_as_uint(x), false, false);
  return fmaxf(__uint_as_float(t[0]), __uint_as_float(t[1]));
}
__device__ __forceinline__ float xrow_sum(float x) {
  auto s = __builtin_amdgcn_permlane16_swap(__float_as_uint(x), __float_as_uint(x), false, false);
  x = __uint_as_float(s[0]) + __uint_as_float(s[1]);
  auto t = __builtin_amdgcn_permlane32_swap(__float_as_uint(x), __float_as_uint(x), false, false);
  return __uint_as_float(t[0]) + __uint_as_float(t[1]);
}
__device__ __forceinline__ float row_sum(float x) {
  x += dpp<XOR1>(x); x += dpp<XOR2>(x); x += dpp<XOR7>(x); x += dpp<XOR8>(x); return x;
}
__device__ __forceinline__ float wave_sum(float x) { return xrow_sum(row_sum(x)); }
__device__ __forceinline__ u32x4 ldnt(const void* p) { return __builtin_nontemporal_load(reinterpret_cast<const u32x4*>(p)); }
__device__ __forceinline__ u32x4 ldpl(const void* p) { return *reinterpret_cast<const u32x4*>(p); }
__device__ __forceinline__ float rdlane(float x, int l) { return __builtin_bit_cast(float, __builtin_amdgcn_readlane(__builtin_bit_cast(int, x), l)); }
#define SB() __builtin_amdgcn_sched_barrier(0)
__device__ __forceinline__ u32x4 poll16(const unsigned char* p) {
  u32x4 v;
  asm volatile("global_load_dwordx4 %0, %1, off sc1\n\ts_waitcnt vmcnt(0)" : "=&v"(v) : "v"(p) : "memory");
  return v;
}

struct State { float m, l; float acc[8]; };

__device__ __forceinline__ float qk_softmax(const u32x4 (&K)[8], const unsigned (&q)[4], State& st, int c) {
  float x[8];
#pragma unroll
  for (int i = 0; i < 8; ++i) {
    float d = dot2(K[i][0], q[0], 0.f);
    d = dot2(K[i][1], q[1], d); d = dot2(K[i][2], q[2], d); d = dot2(K[i][3], q[3], d);
    x[i] = d;
  }
  float y[4], z[2];
#pragma unroll
  for (int j = 0; j < 4; ++j) { const float a = x[j] + dpp<XOR8>(x[j]), b = x[j + 4] + dpp<XOR8>(x[j + 4]); y[j] = (c & 8) ? b : a; }
#pragma unroll
  for (int j = 0; j < 2; ++j) { const float a = y[j] + dpp<XOR7>(y[j]), b = y[j + 2] + dpp<XOR7>(y[j + 2]); z[j] = (c & 4) ? b : a; }
  float s;
  { const float a = z[0] + dpp<XOR2>(z[0]), b = z[1] + dpp<XOR2>(z[1]); s = (c & 2) ? b : a; }
  s += dpp<XOR1>(s);
  s = rbf(rbf(s) * SCALE);
  float tm = fmaxf(s, dpp<XOR2>(s)); tm = fmaxf(tm, dpp<XOR7>(tm)); tm = fmaxf(tm, dpp<XOR8>(tm));
  const float mn = fmaxf(st.m, tm);
  const float alpha = __builtin_amdgcn_exp2f((st.m - mn) * L2E);
  const float p = __builtin_amdgcn_exp2f((s - mn) * L2E);
  st.l = fmaf(st.l, alpha, p); st.m = mn;
#pragma unroll
  for (int k = 0; k < 8; ++k) st.acc[k] *= alpha;
  return p;
}
template <int I> __device__ __forceinline__ void pv_one(const u32x4& V, float p, State& st) {
  const float pb = dpp<0x150 + 2 * I>(p);
#pragma unroll
  for (int k = 0; k < 4; ++k) {
    st.acc[2 * k] = fmaf(pb, bflo(V[k]), st.acc[2 * k]);
    st.acc[2 * k + 1] = fmaf(pb, bfhi(V[k]), st.acc[2 * k + 1]);
  }
}
__device__ __forceinline__ void pv(const u32x4 (&V)[8], float p, State& st) {
  pv_one<0>(V[0], p, st); pv_one<1>(V[1], p, st); pv_one<2>(V[2], p, st); pv_one<3>(V[3], p, st);
  pv_one<4>(V[4], p, st); pv_one<5>(V[5], p, st); pv_one<6>(V[6], p, st); pv_one<7>(V[7], p, st);
  asm volatile("" : "+v"(st.acc[0]), "+v"(st.acc[1]), "+v"(st.acc[2]), "+v"(st.acc[3]),
                    "+v"(st.acc[4]), "+v"(st.acc[5]), "+v"(st.acc[6]), "+v"(st.acc[7]), "+v"(st.l));
}
__device__ __forceinline__ void load_tile(u32x4 (&B)[8], const char* base) {
#pragma unroll
  for (int i = 0; i < 8; ++i) B[i] = ldnt(base + i * 1024);
}
__device__ __forceinline__ void fma8(float (&a)[8], float s, const u32x4& w) {
#pragma unroll
  for (int k = 0; k < 4; ++k) { a[2 * k] = fmaf(s, bflo(w[k]), a[2 * k]); a[2 * k + 1] = fmaf(s, bfhi(w[k]), a[2 * k + 1]); }
}

__global__ void __launch_bounds__(NTHR, 2)
fused_decode(const u16* __restrict__ x, const u16* __restrict__ kc, const u16* __restrict__ vc,
             const u16* __restrict__ wq, const u16* __restrict__ wo, const u16* __restrict__ wg,
             const u16* __restrict__ wu, const u16* __restrict__ wd, u16* __restrict__ out,
             unsigned char* ws) {
  __shared__ __attribute__((aligned(16))) float lds[L_TOTAL];
  const int tid = threadIdx.x, lane = tid & 63, w = __builtin_amdgcn_readfirstlane(tid >> 6);
  const int r = lane >> 4, c = lane & 15;
  const int b = blockIdx.x, g = b & 7, bi = b >> 3;
  const int head = g * 4 + (bi >> 3), sp = bi & 7;
  const bool is_wo = (sp & 3) == 0;
  const int half = sp >> 2;
  const int fl = (bi >> 3) * 6 + (sp < 4 ? sp - 1 : sp - 2);
  const int fidx = g * 24 + fl;
  const __amdgpu_buffer_rsrc_t rs = __builtin_amdgcn_make_buffer_rsrc((void*)ws, 0, (int)WS_END, 0x00020000);

  const unsigned* x32 = reinterpret_cast<const unsigned*>(x);
  unsigned xv[5];
#pragma unroll
  for (int i = 0; i < 4; ++i) xv[i] = x32[lane + 64 * i];
  xv[4] = x32[256 + (lane & 31)];
  const u64 ep = __hip_atomic_load(reinterpret_cast<const u64*>(ws + WS_E), __ATOMIC_RELAXED, __HIP_MEMORY_SCOPE_AGENT);
  u32x4 WQ[18];
  {
    const char* p = reinterpret_cast<const char*>(wq) + ((size_t)(72 * w + 18 * r) * (NH * DH) + head * DH + 8 * c) * 2;
#pragma unroll
    for (int i = 0; i < 18; ++i) WQ[i] = ldpl(p + (size_t)i * (NH * DH * 2));
  }
  const size_t kvoff = ((size_t)head * SK + (size_t)sp * TOK_WG) * (DH * 2) + (size_t)w * 8192 + lane * 16;
  const char* kb = reinterpret_cast<const char*>(kc) + kvoff;
  const char* vb = reinterpret_cast<const char*>(vc) + kvoff;
  u32x4 KA[8], VA[8], KB[8], VB[8];
  load_tile(KA, kb); load_tile(VA, vb);
  SB();

  {
    float xf[10];
#pragma unroll
    for (int i = 0; i < 5; ++i) { xf[2 * i] = bflo(xv[i]); xf[2 * i + 1] = bfhi(xv[i]); }
    const bool tl = lane < 32;
    float s = 0.f;
#pragma unroll
    for (int i = 0; i < 8; ++i) s += xf[i];
    s += tl ? (xf[8] + xf[9]) : 0.f;
    const float mean = wave_sum(s) * (1.f / D);
    float v = 0.f;
#pragma unroll
    for (int i = 0; i < 8; ++i) { const float d = xf[i] - mean; v = fmaf(d, d, v); }
    { const float d0 = xf[8] - mean, d1 = xf[9] - mean; v += tl ? (d0 * d0 + d1 * d1) : 0.f; }
    const float var = wave_sum(v) * (1.f / D);
    const float mb = rbf(mean), den = rbf(sqrtf(rbf(rbf(var) + 1e-5f)));
#pragma unroll
    for (int i = 0; i < 5; ++i) {
      if (i < 4 || tl) {
        const int e = 2 * (lane + 64 * i);
        *reinterpret_cast<f32x2*>(&lds[L_XN + e]) = f32x2{rbf(rbf(xf[2 * i] - mb) / den), rbf(rbf(xf[2 * i + 1] - mb) / den)};
        *reinterpret_cast<f32x2*>(&lds[L_XR + e]) = f32x2{xf[2 * i], xf[2 * i + 1]};
      }
    }
  }
  unsigned q[4];
  {
    float a[8];
#pragma unroll
    for (int k = 0; k < 8; ++k) a[k] = 0.f;
    const float* xn = &lds[L_XN + 72 * w + 18 * r];
#pragma unroll
    for (int i = 0; i < 18; i += 2) {
      const f32x2 xx = *reinterpret_cast<const f32x2*>(xn + i);
      fma8(a, xx[0], WQ[i]); fma8(a, xx[1], WQ[i + 1]);
    }
    SB();
    load_tile(KB, kb + 65536); load_tile(VB, vb + 65536);
    SB();
#pragma unroll
    for (int k = 0; k < 8; ++k) a[k] = xrow_sum(a[k]);
    if (r == 0) {
      *reinterpret_cast<f32x4*>(&lds[L_QP + w * 128 + 8 * c]) = f32x4{a[0], a[1], a[2], a[3]};
      *reinterpret_cast<f32x4*>(&lds[L_QP + w * 128 + 8 * c + 4]) = f32x4{a[4], a[5], a[6], a[7]};
    }
    __syncthreads();
    f32x4 s0 = {0.f, 0.f, 0.f, 0.f}, s1 = {0.f, 0.f, 0.f, 0.f};
#pragma unroll
    for (int j = 0; j < 8; ++j) {
      s0 += *reinterpret_cast<const f32x4*>(&lds[L_QP + j * 128 + 8 * c]);
      s1 += *reinterpret_cast<const f32x4*>(&lds[L_QP + j * 128 + 8 * c + 4]);
    }
    q[0] = packbf(s0[0], s0[1]); q[1] = packbf(s0[2], s0[3]); q[2] = packbf(s1[0], s1[1]); q[3] = packbf(s1[2], s1[3]);
  }

  State st; st.m = -3.0e38f; st.l = 0.f;
#pragma unroll
  for (int k = 0; k < 8; ++k) st.acc[k] = 0.f;
#pragma unroll
  for (int j = 0; j < NTILE - 2; j += 2) {
    float p = qk_softmax(KA, q, st, c); SB();
    load_tile(KA, kb + (size_t)(j + 2) * 65536); SB();
    pv(VA, p, st); SB();
    load_tile(VA, vb + (size_t)(j + 2) * 65536); SB();
    p = qk_softmax(KB, q, st, c); SB();
    load_tile(KB, kb + (size_t)(j + 3) * 65536); SB();
    pv(VB, p, st); SB();
    load_tile(VB, vb + (size_t)(j + 3) * 65536); SB();
  }
  u32x4 X[9], Y[4];
  {
    float p = qk_softmax(KA, q, st, c); SB();
    if (is_wo) {
      const char* pw = reinterpret_cast<const char*>(wo) + (size_t)(head * DH + half * 64 + 8 * w) * (D * 2);
#pragma unroll
      for (int i = 0; i < 8; ++i) X[i] = ldnt(pw + i * (D * 2) + lane * 16);
      X[8] = ldnt(pw + (lane >> 3) * (D * 2) + 1024 + (lane & 7) * 16);
    } else {
      const int cb = (lane & 7) < 6 ? (lane & 7) : 0;
      const char* pg = reinterpret_cast<const char*>(cb < 3 ? wg : wu) + ((size_t)(72 * w + (lane >> 3)) * FF + fidx * 24 + (cb % 3) * 8) * 2;
#pragma unroll
      for (int i = 0; i < 9; ++i) X[i] = ldpl(pg + (size_t)i * (8 * FF * 2));
    }
    SB();
    pv(VA, p, st); SB();
    if (!is_wo) {
      const char* pd = reinterpret_cast<const char*>(wd) + (size_t)(fidx * 24 + 3 * w) * (D * 2);
#pragma unroll
      for (int i = 0; i < 3; ++i) Y[i] = ldnt(pd + i * (D * 2) + lane * 16);
      const int rr = (lane >> 3) < 3 ? (lane >> 3) : 0;
      Y[3] = ldnt(pd + rr * (D * 2) + 1024 + (lane & 7) * 16);
    }
    SB();
    p = qk_softmax(KB, q, st, c); SB();
    pv(VB, p, st); SB();
  }
  const unsigned tag = (unsigned)ep + 1u;

  {
    const float mw = xrow_max(st.m);
    const float f = __builtin_amdgcn_exp2f((st.m - mw) * L2E);
    const float lw = xrow_sum(row_sum(st.l) * (0.5f * f));
#pragma unroll
    for (int k = 0; k < 8; ++k) st.acc[k] = xrow_sum(st.acc[k] * f);
    float* rec = &lds[L_MRG + w * 132];
    if (r == 0) {
      *reinterpret_cast<f32x4*>(rec + 4 + 8 * c) = f32x4{st.acc[0], st.acc[1], st.acc[2], st.acc[3]};
      *reinterpret_cast<f32x4*>(rec + 8 + 8 * c) = f32x4{st.acc[4], st.acc[5], st.acc[6], st.acc[7]};
      if (c == 0) { rec[0] = mw; rec[1] = lw; }
    }
    __syncthreads();
    if (tid < 132) {
      float M = lds[L_MRG];
#pragma unroll
      for (int j = 1; j < 8; ++j) M = fmaxf(M, lds[L_MRG + j * 132]);
      const int src = tid < 128 ? 4 + tid : 1;
      float v = 0.f;
#pragma unroll
      for (int j = 0; j < 8; ++j) v = fmaf(__builtin_amdgcn_exp2f((lds[L_MRG + j * 132] - M) * L2E), lds[L_MRG + j * 132 + src], v);
      int hf, idx;
      if (tid < 128) { hf = tid >> 6; idx = tid & 63; }
      else { hf = (tid - 128) >> 1; idx = 64 + (tid & 1); if (!(tid & 1)) v = M; }
      const unsigned off = WS_IN1 + (unsigned)(((head * 2 + hf) * 8 + sp) * 1024 + idx * 8);
      __builtin_amdgcn_raw_buffer_store_b64(u32x2{__float_as_uint(v), tag}, rs, off, 0, 16);
    }
  }

  float pa[8], pr[8];
#pragma unroll
  for (int k = 0; k < 8; ++k) { pa[k] = 0.f; pr[k] = 0.f; }
  if (is_wo) {
    {
      const unsigned off = WS_IN1 + (unsigned)(((head * 2 + half) * 8 + w) * 1024 + (lane < 33 ? lane : 32) * 16);
      u32x4 gr;
      for (unsigned spins = 0;; ++spins) {
        gr = poll16(ws + off);
        const bool ok = gr[1] == tag && gr[3] == tag;
        if (__all(ok) || spins > SPIN_CAP) break;
        __builtin_amdgcn_s_sleep(1);
        asm volatile("" ::: "memory");
      }
      if (lane < 33) *reinterpret_cast<f32x2*>(&lds[L_REC + w * 68 + 2 * lane]) = f32x2{__uint_as_float(gr[0]), __uint_as_float(gr[2])};
    }
    __syncthreads();
    float M = lds[L_REC + 64];
#pragma unroll
    for (int j = 1; j < 8; ++j) M = fmaxf(M, lds[L_REC + j * 68 + 64]);
    const int js = lane >> 3;
    const float wt = __builtin_amdgcn_exp2f((lds[L_REC + js * 68 + 64] - M) * L2E);
    float o = wt * lds[L_REC + js * 68 + 8 * w + (lane & 7)];
    float L = wt * lds[L_REC + js * 68 + 65];
    o += dpp<XOR8>(o); L += dpp<XOR8>(L);
    o = xrow_sum(o); L = xrow_sum(L);
    o = rbf(o / L);
#pragma unroll
    for (int i = 0; i < 8; ++i) fma8(pa, rdlane(o, i), X[i]);
    const float orow = __shfl(o, lane >> 3);
    fma8(pr, orow, X[8]);
  } else {
    float a[8];
#pragma unroll
    for (int k = 0; k < 8; ++k) a[k] = 0.f;
    const float* xn = &lds[L_XN + 72 * w + (lane >> 3)];
#pragma unroll
    for (int i = 0; i < 9; ++i) fma8(a, xn[8 * i], X[i]);
#pragma unroll
    for (int k = 0; k < 8; ++k) { a[k] += dpp<XOR8>(a[k]); a[k] = xrow_sum(a[k]); }
    if (lane < 6) {
      *reinterpret_cast<f32x4*>(&lds[L_GU + w * 48 + lane * 8]) = f32x4{a[0], a[1], a[2], a[3]};
      *reinterpret_cast<f32x4*>(&lds[L_GU + w * 48 + lane * 8 + 4]) = f32x4{a[4], a[5], a[6], a[7]};
    }
    __syncthreads();
    const int it = (lane & 7) < 6 ? (lane & 7) : 0;
    float s = lds[L_GU + (lane >> 3) * 48 + (it / 3) * 24 + 3 * w + (it % 3)];
    s += dpp<XOR8>(s); s = xrow_sum(s);
    s = rbf(s);
    float h[3];
#pragma unroll
    for (int i = 0; i < 3; ++i) {
      const float gt = rdlane(s, i), up = rdlane(s, 3 + i);
      const float inner = 0.7978845608028654f * (gt + 0.044715f * gt * gt * gt);
      const float ge = rbf(0.5f * gt * (1.f + tanhf(inner)));
      h[i] = rbf(ge * up);
    }
#pragma unroll
    for (int i = 0; i < 3; ++i) fma8(pa, h[i], Y[i]);
    const int rr = lane >> 3;
    const float hrow = rr == 0 ? h[0] : rr == 1 ? h[1] : rr == 2 ? h[2] : 0.f;
    fma8(pr, hrow, Y[3]);
  }
  {
#pragma unroll
    for (int k = 0; k < 8; ++k) { pr[k] += dpp<XOR8>(pr[k]); pr[k] = xrow_sum(pr[k]); }
    float* red = &lds[L_RED + w * 576];
    *reinterpret_cast<f32x4*>(red + 8 * lane) = f32x4{pa[0], pa[1], pa[2], pa[3]};
    *reinterpret_cast<f32x4*>(red + 8 * lane + 4) = f32x4{pa[4], pa[5], pa[6], pa[7]};
    if (lane < 8) {
      *reinterpret_cast<f32x4*>(red + 512 + 8 * lane) = f32x4{pr[0], pr[1], pr[2], pr[3]};
      *reinterpret_cast<f32x4*>(red + 512 + 8 * lane + 4) = f32x4{pr[4], pr[5], pr[6], pr[7]};
    }
    __syncthreads();
    if (tid < 288) {
      f32x2 s = {0.f, 0.f};
#pragma unroll
      for (int j = 0; j < 8; ++j) s += *reinterpret_cast<const f32x2*>(&lds[L_RED + j * 576 + 2 * tid]);
      const int slot = is_wo ? head * 2 + half : 64 + fidx;
      const unsigned off = WS_IN2 + (unsigned)((tid >> 1) * 8192 + slot * 32 + (tid & 1) * 16);
      __builtin_amdgcn_raw_buffer_store_b128(u32x4{__float_as_uint(s[0]), tag, __float_as_uint(s[1]), tag}, rs, off, 0, 16);
    }
  }

  if (!is_wo && fl < 18) {
    const int R = g * 18 + fl;
    const unsigned off = WS_IN2 + (unsigned)(R * 8192 + tid * 16);
    u32x4 gr;
    for (unsigned spins = 0;; ++spins) {
      gr = poll16(ws + off);
      const bool ok = gr[1] == tag && gr[3] == tag;
      if (__all(ok) || spins > SPIN_CAP) break;
      __builtin_amdgcn_s_sleep(1);
      asm volatile("" ::: "memory");
    }
    float v0 = __uint_as_float(gr[0]), v1 = __uint_as_float(gr[2]);
    v0 += dpp<ROR2>(v0); v1 += dpp<ROR2>(v1);
    v0 += dpp<ROR4>(v0); v1 += dpp<ROR4>(v1);
    v0 += dpp<XOR8>(v0); v1 += dpp<XOR8>(v1);
    v0 = xrow_sum(v0); v1 = xrow_sum(v1);
    if (lane < 2) *reinterpret_cast<f32x2*>(&lds[L_FIN + w * 4 + 2 * lane]) = f32x2{v0, v1};
    __syncthreads();
    if (tid < 4) {
      const float A = lds[L_FIN + tid] + lds[L_FIN + 4 + tid];
      float B = 0.f;
#pragma unroll
      for (int j = 2; j < 8; ++j) B += lds[L_FIN + 4 * j + tid];
      const float o = rbf(lds[L_XR + 4 * R + tid] + rbf(A)) + rbf(B);
      out[4 * R + tid] = __builtin_bit_cast(u16, (__bf16)o);
    }
    if (R == 0 && tid == 0)
      __hip_atomic_store(reinterpret_cast<u64*>(ws + WS_E), ep + 1ull, __ATOMIC_RELAXED, __HIP_MEMORY_SCOPE_AGENT);
  }
}

}

extern "C" void kernel_launch(void* const* d_in, const int* in_sizes, int n_in,
                              void* d_out, int out_size, void* d_ws, size_t ws_size,
                              hipStream_t stream) {
  fused_decode<<<NWG, NTHR, 0, stream>>>(
      (const u16*)d_in[0], (const u16*)d_in[1], (const u16*)d_in[2], (const u16*)d_in[3], (const u16*)d_in[4],
      (const u16*)d_in[6], (const u16*)d_in[7], (const u16*)d_in[8], (u16*)d_out, (unsigned char*)d_ws);
}
```

```cpp
#include <hip/hip_runtime.h>
#include <hip/hip_bf16.h>
#include <stdint.h>

namespace {

typedef unsigned u32x4 __attribute__((ext_vector_type(4)));
typedef unsigned u32x2 __attribute__((ext_vector_type(2)));
typedef float f32x4 __attribute__((ext_vector_type(4)));
typedef float f32x2 __attribute__((ext_vector_type(2)));
typedef __bf16 bf16x2_t __attribute__((ext_vector_type(2)));
typedef unsigned short u16;
typedef unsigned long long u64;

constexpr int D = 576, NH = 32, SK = 16384, DH = 128, FF = 4608;
constexpr int NWG = 256, NTHR = 512;
constexpr int NSPLIT = 8, TOK_WG = SK / NSPLIT;
constexpr int NTILE = 64;
#define WQ_AUX1 0
#define WQ_AUX2 0
constexpr int NRED = 144;
constexpr float L2E = 1.4426950408889634f;
constexpr float SCALE = 0.08838834764831845f;

constexpr unsigned WS_E = 0;
constexpr unsigned WS_INF = 4096;
constexpr unsigned WS_IN1 = WS_INF + 128 * 9216;
constexpr unsigned WS_IN2 = WS_IN1 + 32 * 4 * 8 * 512;
constexpr unsigned WS_END = WS_IN2 + NRED * 256 * 32;
constexpr unsigned SPIN_CAP = 1u << 18;

constexpr int L_XN = 0;
constexpr int L_XR = L_XN + 592;
constexpr int L_QP = L_XR + 576;
constexpr int L_MRG = L_QP + 8 * 128;
constexpr int L_REC = L_MRG + 8 * 132;
constexpr int L_GU = L_REC + 8 * 36;
constexpr int L_H = L_GU + 32 * 36;
constexpr int L_RED = L_H + 80;
constexpr int L_FIN = L_RED + 8 * 576;
constexpr int L_CTR = L_FIN + 32;
constexpr int L_TOTAL = L_CTR + 4;

__device__ __forceinline__ float bflo(unsigned u) { return __uint_as_float(u << 16); }
__device__ __forceinline__ float bfhi(unsigned u) { return __uint_as_float(u & 0xffff0000u); }
__device__ __forceinline__ float rbf(float x) { return (float)(__bf16)x; }
__device__ __forceinline__ unsigned packbf(float lo, float hi) {
  bf16x2_t v; v[0] = (__bf16)lo; v[1] = (__bf16)hi; return __builtin_bit_cast(unsigned, v);
}
__device__ __forceinline__ float dot2(unsigned a, unsigned b, float acc) {
  return __builtin_amdgcn_fdot2_f32_bf16(__builtin_bit_cast(bf16x2_t, a), __builtin_bit_cast(bf16x2_t, b), acc, false);
}
template <int CTRL> __device__ __forceinline__ float dpp(float x) {
  return __builtin_bit_cast(float, __builtin_amdgcn_mov_dpp(__builtin_bit_cast(int, x), CTRL, 0xf, 0xf, true));
}
constexpr int XOR1 = 0xB1, XOR2 = 0x4E, XOR7 = 0x141, XOR8 = 0x128, ROR2 = 0x122, ROR4 = 0x124;
__device__ __forceinline__ float xrow_max(float x) {
  auto s = __builtin_amdgcn_permlane16_swap(__float_as_uint(x), __float_as_uint(x), false, false);
  x = fmaxf(__uint_as_float(s[0]), __uint_as_float(s[1]));
  auto t = __builtin_amdgcn_permlane32_swap(__float_as_uint(x), __float_as_uint(x), false, false);
  return fmaxf(__uint_as_float(t[0]), __uint_as_float(t[1]));
}
__device__ __forceinline__ float xrow_sum(float x) {
  auto s = __builtin_amdgcn_permlane16_swap(__float_as_uint(x), __float_as_uint(x), false, false);
  x = __uint_as_float(s[0]) + __uint_as_float(s[1]);
  auto t = __builtin_amdgcn_permlane32_swap(__float_as_uint(x), __float_as_uint(x), false, false);
  return __uint_as_float(t[0]) + __uint_as_float(t[1]);
}
__device__ __forceinline__ float row_sum(float x) {
  x += dpp<XOR1>(x); x += dpp<XOR2>(x); x += dpp<XOR7>(x); x += dpp<XOR8>(x); return x;
}
__device__ __forceinline__ float wave_sum(float x) { return xrow_sum(row_sum(x)); }
__device__ __forceinline__ u32x4 ldnt(const void* p) { return __builtin_nontemporal_load(reinterpret_cast<const u32x4*>(p)); }
__device__ __forceinline__ u32x4 ldpl(const void* p) { return *reinterpret_cast<const u32x4*>(p); }
__device__ __forceinline__ float rdlane(float x, int l) { return __builtin_bit_cast(float, __builtin_amdgcn_readlane(__builtin_bit_cast(int, x), l)); }
#define SB() __builtin_amdgcn_sched_barrier(0)
#define STAMP(i)
__device__ __forceinline__ u32x4 poll16(const unsigned char* p) {
  u32x4 v;
  asm volatile("global_load_dwordx4 %0, %1, off sc1\n\ts_waitcnt vmcnt(0)" : "=&v"(v) : "v"(p) : "memory");
  return v;
}

struct State { float m, l; float acc[8]; };

__device__ __forceinline__ float qk_softmax(const u32x4 (&K)[8], const unsigned (&q)[4], State& st, int c, bool valid) {
  float x[8];
#pragma unroll
  for (int i = 0; i < 8; ++i) {
    float d = dot2(K[i][0], q[0], 0.f);
    d = dot2(K[i][1], q[1], d); d = dot2(K[i][2], q[2], d); d = dot2(K[i][3], q[3], d);
    x[i] = d;
  }
  float y[4], z[2];
#pragma unroll
  for (int j = 0; j < 4; ++j) { const float a = x[j] + dpp<XOR8>(x[j]), b = x[j + 4] + dpp<XOR8>(x[j + 4]); y[j] = (c & 8) ? b : a; }
#pragma unroll
  for (int j = 0; j < 2; ++j) { const float a = y[j] + dpp<XOR7>(y[j]), b = y[j + 2] + dpp<XOR7>(y[j + 2]); z[j] = (c & 4) ? b : a; }
  float s;
  { const float a = z[0] + dpp<XOR2>(z[0]), b = z[1] + dpp<XOR2>(z[1]); s = (c & 2) ? b : a; }
  s += dpp<XOR1>(s);
  s = rbf(rbf(s) * SCALE);
  s = valid ? s : -__builtin_inff();
  float tm = fmaxf(s, dpp<XOR2>(s)); tm = fmaxf(tm, dpp<XOR7>(tm)); tm = fmaxf(tm, dpp<XOR8>(tm));
  const float mn = fmaxf(st.m, tm);
  const float alpha = __builtin_amdgcn_exp2f((st.m - mn) * L2E);
  const float p = __builtin_amdgcn_exp2f((s - mn) * L2E);
  st.l = fmaf(st.l, alpha, p); st.m = mn;
#pragma unroll
  for (int k = 0; k < 8; ++k) st.acc[k] *= alpha;
  return p;
}
template <int I> __device__ __forceinline__ void pv_one(const u32x4& V, float p, State& st) {
  const float pb = dpp<0x150 + 2 * I>(p);
#pragma unroll
  for (int k = 0; k < 4; ++k) {
    st.acc[2 * k] = fmaf(pb, bflo(V[k]), st.acc[2 * k]);
    st.acc[2 * k + 1] = fmaf(pb, bfhi(V[k]), st.acc[2 * k + 1]);
  }
}
__device__ __forceinline__ void pv(const u32x4 (&V)[8], float p, State& st) {
  pv_one<0>(V[0], p, st); pv_one<1>(V[1], p, st); pv_one<2>(V[2], p, st); pv_one<3>(V[3], p, st);
  pv_one<4>(V[4], p, st); pv_one<5>(V[5], p, st); pv_one<6>(V[6], p, st); pv_one<7>(V[7], p, st);
  asm volatile("" : "+v"(st.acc[0]), "+v"(st.acc[1]), "+v"(st.acc[2]), "+v"(st.acc[3]),
                    "+v"(st.acc[4]), "+v"(st.acc[5]), "+v"(st.acc[6]), "+v"(st.acc[7]), "+v"(st.l));
}
__device__ __forceinline__ void load_tile(u32x4 (&B)[8], __amdgpu_buffer_rsrc_t rsrc, unsigned voff) {
#pragma unroll
  for (int i = 0; i < 8; ++i) B[i] = __builtin_amdgcn_raw_buffer_load_b128(rsrc, voff + i * 1024, 0, 2);
}
__device__ __forceinline__ void fma8(float (&a)[8], float s, const u32x4& w) {
#pragma unroll
  for (int k = 0; k < 4; ++k) { a[2 * k] = fmaf(s, bflo(w[k]), a[2 * k]); a[2 * k + 1] = fmaf(s, bfhi(w[k]), a[2 * k + 1]); }
}

struct Rows { u32x4 m[5]; u32x4 rem; };
__device__ __forceinline__ void load_rows(Rows& R, __amdgpu_buffer_rsrc_t rsrc, unsigned off, unsigned stride, int nr, int lane) {
  constexpr unsigned OOB = 0x40000000u;
#pragma unroll
  for (int i = 0; i < 5; ++i)
    R.m[i] = __builtin_amdgcn_raw_buffer_load_b128(rsrc, (i < nr ? off + i * stride : OOB) + lane * 16, 0, 2);
  R.rem = __builtin_amdgcn_raw_buffer_load_b128(rsrc, ((lane >> 3) < nr ? off + (lane >> 3) * stride : OOB) + 1024 + (lane & 7) * 16, 0, 2);
}

__global__ void __launch_bounds__(NTHR, 2)
fused_decode(const u16* __restrict__ x, const u16* __restrict__ kc, const u16* __restrict__ vc,
             const u16* __restrict__ wq, const u16* __restrict__ wo, const u16* __restrict__ wg,
             const u16* __restrict__ wu, const u16* __restrict__ wd, u16* __restrict__ out,
             unsigned char* ws) {
  __shared__ __attribute__((aligned(16))) float lds[L_TOTAL];
  const int tid = threadIdx.x, lane = tid & 63, w = __builtin_amdgcn_readfirstlane(tid >> 6);
  const int r = lane >> 4, c = lane & 15;
  const int b = blockIdx.x, g = b & 7, bi = b >> 3;
  const int head = g * 4 + (bi >> 3), sp = bi & 7;
  const bool is_wo = (sp & 1) == 0;
  const int quarter = sp >> 1;
  const int fidx = g * 16 + (bi >> 3) * 4 + quarter;
  const int f1m = b >> 7, f1c = (b >> 4) & 7, f1r = b & 15;
  const int r36 = w < 4 ? 5 * w : 4 * w + 4, n36 = w < 4 ? 5 : 4;
  const __amdgpu_buffer_rsrc_t rs = __builtin_amdgcn_make_buffer_rsrc((void*)ws, 0, (int)WS_END, 0x00020000);

  STAMP(0);
  const unsigned* x32 = reinterpret_cast<const unsigned*>(x);
  unsigned xv[5];
#pragma unroll
  for (int i = 0; i < 4; ++i) xv[i] = x32[lane + 64 * i];
  xv[4] = x32[256 + (lane & 31)];
  const u64 ep = __hip_atomic_load(reinterpret_cast<const u64*>(ws + WS_E), __ATOMIC_RELAXED, __HIP_MEMORY_SCOPE_AGENT);
  const __amdgpu_buffer_rsrc_t rq = __builtin_amdgcn_make_buffer_rsrc((void*)(wq + head * DH), 0, (D * NH * DH - head * DH) * 2, 0x00020000);
  const int i0 = (18 * sp) >> 3;
  const unsigned qoff = (unsigned)((4 * w + r) * (NH * DH * 2) + c * 16);
  u32x4 WQ[18];
#pragma unroll
  for (int j = 0; j < 3; ++j) WQ[j] = __builtin_amdgcn_raw_buffer_load_b128(rq, qoff, (unsigned)(i0 + j) * (32 * NH * DH * 2), WQ_AUX1);
  SB();
  Rows F1, X;
  {
    const __amdgpu_buffer_rsrc_t rg = __builtin_amdgcn_make_buffer_rsrc((void*)(f1m ? wu : wg), 0, D * FF * 2, 0x00020000);
    load_rows(F1, rg, (unsigned)(((f1r * 36 + r36) * FF + f1c * 576) * 2), FF * 2, n36, lane);
    const __amdgpu_buffer_rsrc_t rx = __builtin_amdgcn_make_buffer_rsrc((void*)(is_wo ? wo : wd), 0, (is_wo ? NH * DH : FF) * D * 2, 0x00020000);
    const int row0 = is_wo ? head * DH + quarter * 32 + 4 * w : fidx * 36 + r36;
    load_rows(X, rx, (unsigned)(row0 * (D * 2)), D * 2, is_wo ? 4 : n36, lane);
  }
  const size_t kvoff = ((size_t)head * SK + (size_t)sp * TOK_WG) * (DH * 2);
  const __amdgpu_buffer_rsrc_t rk = __builtin_amdgcn_make_buffer_rsrc((void*)(reinterpret_cast<const char*>(kc) + kvoff), 0, TOK_WG * DH * 2, 0x00020000);
  const __amdgpu_buffer_rsrc_t rv = __builtin_amdgcn_make_buffer_rsrc((void*)(reinterpret_cast<const char*>(vc) + kvoff), 0, TOK_WG * DH * 2, 0x00020000);
  const unsigned l16 = lane * 16;
  if (tid == 0) reinterpret_cast<unsigned*>(lds)[L_CTR] = 16u;
  SB();

  {
    float xf[10];
#pragma unroll
    for (int i = 0; i < 5; ++i) { xf[2 * i] = bflo(xv[i]); xf[2 * i + 1] = bfhi(xv[i]); }
    const bool tl = lane < 32;
    float s = 0.f;
#pragma unroll
    for (int i = 0; i < 8; ++i) s += xf[i];
    s += tl ? (xf[8] + xf[9]) : 0.f;
    const float mean = wave_sum(s) * (1.f / D);
    float v = 0.f;
#pragma unroll
    for (int i = 0; i < 8; ++i) { const float d = xf[i] - mean; v = fmaf(d, d, v); }
    { const float d0 = xf[8] - mean, d1 = xf[9] - mean; v += tl ? (d0 * d0 + d1 * d1) : 0.f; }
    const float var = wave_sum(v) * (1.f / D);
    const float mb = rbf(mean), den = rbf(sqrtf(rbf(rbf(var) + 1e-5f)));
#pragma unroll
    for (int i = 0; i < 5; ++i) {
      if (i < 4 || tl) {
        const int e = 2 * (lane + 64 * i);
        *reinterpret_cast<f32x2*>(&lds[L_XN + e]) = f32x2{rbf(rbf(xf[2 * i] - mb) / den), rbf(rbf(xf[2 * i + 1] - mb) / den)};
        *reinterpret_cast<f32x2*>(&lds[L_XR + e]) = f32x2{xf[2 * i], xf[2 * i + 1]};
      }
    }
  }
  STAMP(7);
  const unsigned tag = (unsigned)ep + 1u;
  float qa[8];
#pragma unroll
  for (int k = 0; k < 8; ++k) qa[k] = 0.f;
  const float* xq = &lds[L_XN + 4 * w + r];
#pragma unroll
  for (int j = 0; j < 3; ++j) fma8(qa, xq[32 * (i0 + j)], WQ[j]);
  asm volatile("" : "+v"(qa[0]), "+v"(qa[1]), "+v"(qa[2]), "+v"(qa[3]), "+v"(qa[4]), "+v"(qa[5]), "+v"(qa[6]), "+v"(qa[7]));
  SB();
#pragma unroll
  for (int j = 3; j < 18; ++j) {
    const int i = i0 + j < 18 ? i0 + j : i0 + j - 18;
    WQ[j] = __builtin_amdgcn_raw_buffer_load_b128(rq, qoff, (unsigned)i * (32 * NH * DH * 2), WQ_AUX2);
  }
  u32x4 KA[8], VA[8], KB[8], VB[8];
  SB(); load_tile(KA, rk, l16 + w * 8192); SB(); load_tile(VA, rv, l16 + w * 8192); SB();
  STAMP(8);
  auto rows_to_lds = [&](const Rows& R, const float (&cf)[5], float crem) {
    float pa[8], pr[8];
#pragma unroll
    for (int k = 0; k < 8; ++k) { pa[k] = 0.f; pr[k] = 0.f; }
#pragma unroll
    for (int i = 0; i < 5; ++i) fma8(pa, cf[i], R.m[i]);
    fma8(pr, crem, R.rem);
#pragma unroll
    for (int k = 0; k < 8; ++k) { pr[k] += dpp<XOR8>(pr[k]); pr[k] = xrow_sum(pr[k]); }
    float* red = &lds[L_RED + w * 576];
    *reinterpret_cast<f32x4*>(red + 8 * lane) = f32x4{pa[0], pa[1], pa[2], pa[3]};
    *reinterpret_cast<f32x4*>(red + 8 * lane + 4) = f32x4{pa[4], pa[5], pa[6], pa[7]};
    if (lane < 8) {
      *reinterpret_cast<f32x4*>(red + 512 + 8 * lane) = f32x4{pr[0], pr[1], pr[2], pr[3]};
      *reinterpret_cast<f32x4*>(red + 512 + 8 * lane + 4) = f32x4{pr[4], pr[5], pr[6], pr[7]};
    }
  };
  auto red_sum = [&]() -> u32x4 {
    f32x2 s = {0.f, 0.f};
#pragma unroll
    for (int j = 0; j < 8; ++j) s += *reinterpret_cast<const f32x2*>(&lds[L_RED + j * 576 + 2 * tid]);
    return u32x4{__float_as_uint(s[0]), tag, __float_as_uint(s[1]), tag};
  };
  {
    const float* xr = &lds[L_XN + f1r * 36 + r36];
    float cf[5];
#pragma unroll
    for (int i = 0; i < 5; ++i) cf[i] = xr[i < n36 ? i : 0];
    rows_to_lds(F1, cf, xr[(lane >> 3) < n36 ? (lane >> 3) : 0]);
  }
  unsigned q[4];
  {
#pragma unroll
    for (int j = 3; j < 18; ++j) {
      const int i = i0 + j < 18 ? i0 + j : i0 + j - 18;
      fma8(qa, xq[32 * i], WQ[j]);
    }
    asm volatile("" : "+v"(qa[0]), "+v"(qa[1]), "+v"(qa[2]), "+v"(qa[3]), "+v"(qa[4]), "+v"(qa[5]), "+v"(qa[6]), "+v"(qa[7]));
    SB(); load_tile(KB, rk, l16 + (8 + w) * 8192); SB(); load_tile(VB, rv, l16 + (8 + w) * 8192); SB();
#pragma unroll
    for (int k = 0; k < 8; ++k) qa[k] = xrow_sum(qa[k]);
    if (r == 0) {
      *reinterpret_cast<f32x4*>(&lds[L_QP + w * 128 + 8 * c]) = f32x4{qa[0], qa[1], qa[2], qa[3]};
      *reinterpret_cast<f32x4*>(&lds[L_QP + w * 128 + 8 * c + 4]) = f32x4{qa[4], qa[5], qa[6], qa[7]};
    }
    __syncthreads();
    if (tid < 288) {
      const int col = 2 * tid, cons = f1c * 16 + col / 36;
      const unsigned off = WS_INF + (unsigned)(cons * 9216 + (f1m * 16 + f1r) * 288 + (col % 36) * 8);
      __builtin_amdgcn_raw_buffer_store_b128(red_sum(), rs, off, 0, 16);
    }
    f32x4 s0 = {0.f, 0.f, 0.f, 0.f}, s1 = {0.f, 0.f, 0.f, 0.f};
#pragma unroll
    for (int j = 0; j < 8; ++j) {
      s0 += *reinterpret_cast<const f32x4*>(&lds[L_QP + j * 128 + 8 * c]);
      s1 += *reinterpret_cast<const f32x4*>(&lds[L_QP + j * 128 + 8 * c + 4]);
    }
    q[0] = packbf(s0[0], s0[1]); q[1] = packbf(s0[2], s0[3]); q[2] = packbf(s1[0], s1[1]); q[3] = packbf(s1[2], s1[3]);
  }

  STAMP(1);
  State st; st.m = -3.0e38f; st.l = 0.f;
#pragma unroll
  for (int k = 0; k < 8; ++k) st.acc[k] = 0.f;
  auto grab = [&]() -> int {
    unsigned t = 0;
    if (lane == 0) t = __hip_atomic_fetch_add(&reinterpret_cast<unsigned*>(lds)[L_CTR], 1u, __ATOMIC_RELAXED, __HIP_MEMORY_SCOPE_WORKGROUP);
    return __builtin_amdgcn_readfirstlane(t);
  };
  int tA = 0, tB = 0;
  do {
    const int tA2 = grab();
    float p = qk_softmax(KA, q, st, c, true); SB();
    load_tile(KA, rk, l16 + tA2 * 8192); SB();
    pv(VA, p, st); SB();
    load_tile(VA, rv, l16 + tA2 * 8192); SB();
    const int tB2 = grab();
    p = qk_softmax(KB, q, st, c, tB < NTILE); SB();
    load_tile(KB, rk, l16 + tB2 * 8192); SB();
    pv(VB, p, st); SB();
    load_tile(VB, rv, l16 + tB2 * 8192); SB();
    tA = tA2; tB = tB2;
  } while (tA < NTILE);
  STAMP(2);

  {
    const float mw = xrow_max(st.m);
    const float f = __builtin_amdgcn_exp2f((st.m - mw) * L2E);
    const float lw = xrow_sum(row_sum(st.l) * (0.5f * f));
#pragma unroll
    for (int k = 0; k < 8; ++k) st.acc[k] = xrow_sum(st.acc[k] * f);
    float* rec = &lds[L_MRG + w * 132];
    if (r == 0) {
      *reinterpret_cast<f32x4*>(rec + 4 + 8 * c) = f32x4{st.acc[0], st.acc[1], st.acc[2], st.acc[3]};
      *reinterpret_cast<f32x4*>(rec + 8 + 8 * c) = f32x4{st.acc[4], st.acc[5], st.acc[6], st.acc[7]};
      if (c == 0) { rec[0] = mw; rec[1] = lw; }
    }
    __syncthreads();
    if (tid < 136) {
      float M = lds[L_MRG];
#pragma unroll
      for (int j = 1; j < 8; ++j) M = fmaxf(M, lds[L_MRG + j * 132]);
      const int src = tid < 128 ? 4 + tid : 1;
      float v = 0.f;
#pragma unroll
      for (int j = 0; j < 8; ++j) v = fmaf(__builtin_amdgcn_exp2f((lds[L_MRG + j * 132] - M) * L2E), lds[L_MRG + j * 132 + src], v);
      int qt, idx;
      if (tid < 128) { qt = tid >> 5; idx = tid & 31; }
      else { qt = (tid - 128) >> 1; idx = 32 + (tid & 1); if (!(tid & 1)) v = M; }
      const unsigned off = WS_IN1 + (unsigned)(((head * 4 + qt) * 8 + sp) * 512 + idx * 8);
      __builtin_amdgcn_raw_buffer_store_b64(u32x2{__float_as_uint(v), tag}, rs, off, 0, 16);
    }
  }
  STAMP(3);

  float cf[5], crem;
  if (is_wo) {
    {
      const unsigned off = WS_IN1 + (unsigned)(((head * 4 + quarter) * 8 + w) * 512 + (lane < 17 ? lane : 16) * 16);
      u32x4 gr;
      for (unsigned spins = 0;; ++spins) {
        gr = poll16(ws + off);
        const bool ok = gr[1] == tag && gr[3] == tag;
        if (__all(ok) || spins > SPIN_CAP) break;
        __builtin_amdgcn_s_sleep(1);
      }
      if (lane < 17) *reinterpret_cast<f32x2*>(&lds[L_REC + w * 36 + 2 * lane]) = f32x2{__uint_as_float(gr[0]), __uint_as_float(gr[2])};
    }
    __syncthreads();
    float M = lds[L_REC + 32];
#pragma unroll
    for (int j = 1; j < 8; ++j) M = fmaxf(M, lds[L_REC + j * 36 + 32]);
    const int js = lane >> 3;
    const float wt = __builtin_amdgcn_exp2f((lds[L_REC + js * 36 + 32] - M) * L2E);
    float o = wt * lds[L_REC + js * 36 + 4 * w + (lane & 3)];
    float L = wt * lds[L_REC + js * 36 + 33];
    o += dpp<XOR8>(o); L += dpp<XOR8>(L);
    o = xrow_sum(o); L = xrow_sum(L);
    o = rbf(o / L);
#pragma unroll
    for (int i = 0; i < 4; ++i) cf[i] = rdlane(o, i);
    cf[4] = 0.f;
    crem = __shfl(o, (lane >> 3) & 3);
  } else {
    {
      const unsigned char* pf = ws + WS_INF + fidx * 9216;
      u32x4 gr;
      for (unsigned spins = 0;; ++spins) {
        gr = poll16(pf + tid * 16);
        const bool ok = gr[1] == tag && gr[3] == tag;
        if (__all(ok) || spins > SPIN_CAP) break;
        __builtin_amdgcn_s_sleep(1);
      }
      *reinterpret_cast<f32x2*>(&lds[L_GU + 2 * tid]) = f32x2{__uint_as_float(gr[0]), __uint_as_float(gr[2])};
      if (w == 0) {
        for (unsigned spins = 0;; ++spins) {
          gr = poll16(pf + 8192 + lane * 16);
          const bool ok = gr[1] == tag && gr[3] == tag;
          if (__all(ok) || spins > SPIN_CAP) break;
          __builtin_amdgcn_s_sleep(1);
        }
        *reinterpret_cast<f32x2*>(&lds[L_GU + 1024 + 2 * lane]) = f32x2{__uint_as_float(gr[0]), __uint_as_float(gr[2])};
      }
    }
    __syncthreads();
    const int it = (lane & 7) < 5 ? (lane & 7) : 0;
    const int col = r36 + it < 36 ? r36 + it : 35;
    const float* src = &lds[L_GU + (lane >> 3) * 4 * 36 + col];
    float s = (src[0] + src[36]) + (src[72] + src[108]);
    s += dpp<XOR8>(s);
    { auto t = __builtin_amdgcn_permlane16_swap(__float_as_uint(s), __float_as_uint(s), false, false); s = __uint_as_float(t[0]) + __uint_as_float(t[1]); }
    s = rbf(s);
    const float up = __shfl(s, (lane & 7) + 32), gt = __shfl(s, lane & 7);
    const float inner = 0.7978845608028654f * (gt + 0.044715f * gt * gt * gt);
    const float ge = rbf(0.5f * gt * (1.f + tanhf(inner)));
    const float h = rbf(ge * up);
#pragma unroll
    for (int i = 0; i < 5; ++i) cf[i] = rdlane(h, i);
    crem = __shfl(h, (lane >> 3) < 5 ? (lane >> 3) : 0);
  }
  STAMP(4);
  rows_to_lds(X, cf, crem);
  __syncthreads();
  if (tid < 288) {
    const int slot = is_wo ? head * 4 + quarter : 128 + fidx;
    const unsigned off = WS_IN2 + (unsigned)((tid >> 1) * 8192 + slot * 32 + (tid & 1) * 16);
    __builtin_amdgcn_raw_buffer_store_b128(red_sum(), rs, off, 0, 16);
  }
  STAMP(5);

  if (b < NRED) {
    const int R = b;
    const unsigned off = WS_IN2 + (unsigned)(R * 8192 + tid * 16);
    u32x4 gr;
    for (unsigned spins = 0;; ++spins) {
      gr = poll16(ws + off);
      const bool ok = gr[1] == tag && gr[3] == tag;
      if (__all(ok) || spins > SPIN_CAP) break;
      __builtin_amdgcn_s_sleep(1);
    }
    float v0 = __uint_as_float(gr[0]), v1 = __uint_as_float(gr[2]);
    v0 += dpp<ROR2>(v0); v1 += dpp<ROR2>(v1);
    v0 += dpp<ROR4>(v0); v1 += dpp<ROR4>(v1);
    v0 += dpp<XOR8>(v0); v1 += dpp<XOR8>(v1);
    v0 = xrow_sum(v0); v1 = xrow_sum(v1);
    if (lane < 2) *reinterpret_cast<f32x2*>(&lds[L_FIN + w * 4 + 2 * lane]) = f32x2{v0, v1};
    __syncthreads();
    if (tid < 4) {
      const float A = (lds[L_FIN + tid] + lds[L_FIN + 4 + tid]) + (lds[L_FIN + 8 + tid] + lds[L_FIN + 12 + tid]);
      const float B = (lds[L_FIN + 16 + tid] + lds[L_FIN + 20 + tid]) + (lds[L_FIN + 24 + tid] + lds[L_FIN + 28 + tid]);
      const float o = rbf(lds[L_XR + 4 * R + tid] + rbf(A)) + rbf(B);
      out[4 * R + tid] = __builtin_bit_cast(u16, (__bf16)o);
    }
    if (R == 0 && tid == 0)
      __hip_atomic_store(reinterpret_cast<u64*>(ws + WS_E), ep + 1ull, __ATOMIC_RELAXED, __HIP_MEMORY_SCOPE_AGENT);
  }
  STAMP(6);
}

}

extern "C" void kernel_launch(void* const* d_in, const int* in_sizes, int n_in,
                              void* d_out, int out_size, void* d_ws, size_t ws_size,
                              hipStream_t stream) {
  fused_decode<<<NWG, NTHR, 0, stream>>>(
      (const u16*)d_in[0], (const u16*)d_in[1], (const u16*)d_in[2], (const u16*)d_in[3], (const u16*)d_in[4],
      (const u16*)d_in[6], (const u16*)d_in[7], (const u16*)d_in[8], (u16*)d_out, (unsigned char*)d_ws);
}
```

```cpp
#include <hip/hip_runtime.h>
#include <hip/hip_bf16.h>
#include <stdint.h>

namespace {

typedef unsigned u32x4 __attribute__((ext_vector_type(4)));
typedef unsigned u32x2 __attribute__((ext_vector_type(2)));
typedef float f32x4 __attribute__((ext_vector_type(4)));
typedef float f32x2 __attribute__((ext_vector_type(2)));
typedef __bf16 bf16x2_t __attribute__((ext_vector_type(2)));
typedef unsigned short u16;
typedef unsigned long long u64;

constexpr int D = 576, NH = 32, SK = 16384, DH = 128, FF = 4608;
constexpr int NWG = 256, NTHR = 512;
constexpr int NSPLIT = 8, TOK_WG = SK / NSPLIT;
constexpr int NTILE = 64;
#define PRE2 0
constexpr int NRED = 144;
constexpr float L2E = 1.4426950408889634f;
constexpr float SCALE = 0.08838834764831845f;

constexpr unsigned WS_E = 0;
constexpr unsigned WS_IN0 = 4096;
constexpr unsigned WS_INF = WS_IN0 + 32 * 8 * 1024;
constexpr unsigned WS_IN1 = WS_INF + 128 * 9216;
constexpr unsigned WS_IN2 = WS_IN1 + 32 * 4 * 8 * 512;
constexpr unsigned WS_END = WS_IN2 + NRED * 256 * 32;
constexpr unsigned SPIN_CAP = 1u << 18;

constexpr int L_XN = 0;
constexpr int L_XR = L_XN + 592;
constexpr int L_QP = L_XR + 576;
constexpr int L_MRG = L_QP + 8 * 128;
constexpr int L_REC = L_MRG + 8 * 132;
constexpr int L_GU = L_REC + 8 * 36;
constexpr int L_H = L_GU + 32 * 36;
constexpr int L_RED = L_H + 80;
constexpr int L_FIN = L_RED + 8 * 576;
constexpr int L_QS = L_FIN + 32;
constexpr int L_CTR = L_QS + 64;
constexpr int L_TOTAL = L_CTR + 4;

__device__ __forceinline__ float bflo(unsigned u) { return __uint_as_float(u << 16); }
__device__ __forceinline__ float bfhi(unsigned u) { return __uint_as_float(u & 0xffff0000u); }
__device__ __forceinline__ float rbf(float x) { return (float)(__bf16)x; }
__device__ __forceinline__ unsigned packbf(float lo, float hi) {
  bf16x2_t v; v[0] = (__bf16)lo; v[1] = (__bf16)hi; return __builtin_bit_cast(unsigned, v);
}
__device__ __forceinline__ float dot2(unsigned a, unsigned b, float acc) {
  return __builtin_amdgcn_fdot2_f32_bf16(__builtin_bit_cast(bf16x2_t, a), __builtin_bit_cast(bf16x2_t, b), acc, false);
}
template <int CTRL> __device__ __forceinline__ float dpp(float x) {
  return __builtin_bit_cast(float, __builtin_amdgcn_mov_dpp(__builtin_bit_cast(int, x), CTRL, 0xf, 0xf, true));
}
constexpr int XOR1 = 0xB1, XOR2 = 0x4E, XOR7 = 0x141, XOR8 = 0x128, ROR2 = 0x122, ROR4 = 0x124;
__device__ __forceinline__ float xrow_max(float x) {
  auto s = __builtin_amdgcn_permlane16_swap(__float_as_uint(x), __float_as_uint(x), false, false);
  x = fmaxf(__uint_as_float(s[0]), __uint_as_float(s[1]));
  auto t = __builtin_amdgcn_permlane32_swap(__float_as_uint(x), __float_as_uint(x), false, false);
  return fmaxf(__uint_as_float(t[0]), __uint_as_float(t[1]));
}
__device__ __forceinline__ float xrow_sum(float x) {
  auto s = __builtin_amdgcn_permlane16_swap(__float_as_uint(x), __float_as_uint(x), false, false);
  x = __uint_as_float(s[0]) + __uint_as_float(s[1]);
  auto t = __builtin_amdgcn_permlane32_swap(__float_as_uint(x), __float_as_uint(x), false, false);
  return __uint_as_float(t[0]) + __uint_as_float(t[1]);
}
__device__ __forceinline__ float row_sum(float x) {
  x += dpp<XOR1>(x); x += dpp<XOR2>(x); x += dpp<XOR7>(x); x += dpp<XOR8>(x); return x;
}
__device__ __forceinline__ float wave_sum(float x) { return xrow_sum(row_sum(x)); }
__device__ __forceinline__ u32x4 ldnt(const void* p) { return __builtin_nontemporal_load(reinterpret_cast<const u32x4*>(p)); }
__device__ __forceinline__ u32x4 ldpl(const void* p) { return *reinterpret_cast<const u32x4*>(p); }
__device__ __forceinline__ float rdlane(float x, int l) { return __builtin_bit_cast(float, __builtin_amdgcn_readlane(__builtin_bit_cast(int, x), l)); }
#define SB() __builtin_amdgcn_sched_barrier(0)
#define STAMP(i)
__device__ __forceinline__ u32x4 poll16(const unsigned char* p) {
  u32x4 v;
  asm volatile("global_load_dwordx4 %0, %1, off sc1\n\ts_waitcnt vmcnt(0)" : "=&v"(v) : "v"(p) : "memory");
  return v;
}

struct State { float m, l; float acc[8]; };

__device__ __forceinline__ float qk_softmax(const u32x4 (&K)[8], const unsigned (&q)[4], State& st, int c, bool valid) {
  float x[8];
#pragma unroll
  for (int i = 0; i < 8; ++i) {
    float d = dot2(K[i][0], q[0], 0.f);
    d = dot2(K[i][1], q[1], d); d = dot2(K[i][2], q[2], d); d = dot2(K[i][3], q[3], d);
    x[i] = d;
  }
  float y[4], z[2];
#pragma unroll
  for (int j = 0; j < 4; ++j) { const float a = x[j] + dpp<XOR8>(x[j]), b = x[j + 4] + dpp<XOR8>(x[j + 4]); y[j] = (c & 8) ? b : a; }
#pragma unroll
  for (int j = 0; j < 2; ++j) { const float a = y[j] + dpp<XOR7>(y[j]), b = y[j + 2] + dpp<XOR7>(y[j + 2]); z[j] = (c & 4) ? b : a; }
  float s;
  { const float a = z[0] + dpp<XOR2>(z[0]), b = z[1] + dpp<XOR2>(z[1]); s = (c & 2) ? b : a; }
  s += dpp<XOR1>(s);
  s = rbf(rbf(s) * SCALE);
  s = valid ? s : -__builtin_inff();
  float tm = fmaxf(s, dpp<XOR2>(s)); tm = fmaxf(tm, dpp<XOR7>(tm)); tm = fmaxf(tm, dpp<XOR8>(tm));
  const float mn = fmaxf(st.m, tm);
  const float alpha = __builtin_amdgcn_exp2f((st.m - mn) * L2E);
  const float p = __builtin_amdgcn_exp2f((s - mn) * L2E);
  st.l = fmaf(st.l, alpha, p); st.m = mn;
#pragma unroll
  for (int k = 0; k < 8; ++k) st.acc[k] *= alpha;
  return p;
}
template <int I> __device__ __forceinline__ void pv_one(const u32x4& V, float p, State& st) {
  const float pb = dpp<0x150 + 2 * I>(p);
#pragma unroll
  for (int k = 0; k < 4; ++k) {
    st.acc[2 * k] = fmaf(pb, bflo(V[k]), st.acc[2 * k]);
    st.acc[2 * k + 1] = fmaf(pb, bfhi(V[k]), st.acc[2 * k + 1]);
  }
}
__device__ __forceinline__ void pv(const u32x4 (&V)[8], float p, State& st) {
  pv_one<0>(V[0], p, st); pv_one<1>(V[1], p, st); pv_one<2>(V[2], p, st); pv_one<3>(V[3], p, st);
  pv_one<4>(V[4], p, st); pv_one<5>(V[5], p, st); pv_one<6>(V[6], p, st); pv_one<7>(V[7], p, st);
  asm volatile("" : "+v"(st.acc[0]), "+v"(st.acc[1]), "+v"(st.acc[2]), "+v"(st.acc[3]),
                    "+v"(st.acc[4]), "+v"(st.acc[5]), "+v"(st.acc[6]), "+v"(st.acc[7]), "+v"(st.l));
}
__device__ __forceinline__ void load_tile(u32x4 (&B)[8], __amdgpu_buffer_rsrc_t rsrc, unsigned voff) {
#pragma unroll
  for (int i = 0; i < 8; ++i) B[i] = __builtin_amdgcn_raw_buffer_load_b128(rsrc, voff + i * 1024, 0, 2);
}
__device__ __forceinline__ void fma8(float (&a)[8], float s, const u32x4& w) {
#pragma unroll
  for (int k = 0; k < 4; ++k) { a[2 * k] = fmaf(s, bflo(w[k]), a[2 * k]); a[2 * k + 1] = fmaf(s, bfhi(w[k]), a[2 * k + 1]); }
}

struct Rows { u32x4 m[5]; u32x4 rem; };
__device__ __forceinline__ void load_rows(Rows& R, __amdgpu_buffer_rsrc_t rsrc, unsigned off, unsigned stride, int nr, int lane) {
  constexpr unsigned OOB = 0x40000000u;
#pragma unroll
  for (int i = 0; i < 5; ++i)
    R.m[i] = __builtin_amdgcn_raw_buffer_load_b128(rsrc, (i < nr ? off + i * stride : OOB) + lane * 16, 0, 2);
  R.rem = __builtin_amdgcn_raw_buffer_load_b128(rsrc, ((lane >> 3) < nr ? off + (lane >> 3) * stride : OOB) + 1024 + (lane & 7) * 16, 0, 2);
}

__global__ void __launch_bounds__(NTHR, 2)
fused_decode(const u16* __restrict__ x, const u16* __restrict__ kc, const u16* __restrict__ vc,
             const u16* __restrict__ wq, const u16* __restrict__ wo, const u16* __restrict__ wg,
             const u16* __restrict__ wu, const u16* __restrict__ wd, u16* __restrict__ out,
             unsigned char* ws) {
  __shared__ __attribute__((aligned(16))) float lds[L_TOTAL];
  const int tid = threadIdx.x, lane = tid & 63, w = __builtin_amdgcn_readfirstlane(tid >> 6);
  const int r = lane >> 4, c = lane & 15;
  const int b = blockIdx.x, g = b & 7, bi = b >> 3;
  const int head = g * 4 + (bi >> 3), sp = bi & 7;
  const bool is_wo = (sp & 1) == 0;
  const int quarter = sp >> 1;
  const int fidx = g * 16 + (bi >> 3) * 4 + quarter;
  const int f1m = b >> 7, f1c = (b >> 4) & 7, f1r = b & 15;
  const int r36 = w < 4 ? 5 * w : 4 * w + 4, n36 = w < 4 ? 5 : 4;
  const __amdgpu_buffer_rsrc_t rs = __builtin_amdgcn_make_buffer_rsrc((void*)ws, 0, (int)WS_END, 0x00020000);

  STAMP(0);
  const unsigned* x32 = reinterpret_cast<const unsigned*>(x);
  unsigned xv[5];
#pragma unroll
  for (int i = 0; i < 4; ++i) xv[i] = x32[lane + 64 * i];
  xv[4] = x32[256 + (lane & 31)];
  const u64 ep = __hip_atomic_load(reinterpret_cast<const u64*>(ws + WS_E), __ATOMIC_RELAXED, __HIP_MEMORY_SCOPE_AGENT);
  u32x4 WQ[3];
  {
    const char* p = reinterpret_cast<const char*>(wq) + ((size_t)(72 * sp + (tid >> 4)) * (NH * DH) + head * DH + 8 * c) * 2;
    WQ[0] = ldnt(p); WQ[1] = ldnt(p + (size_t)32 * (NH * DH * 2));
    WQ[2] = ldnt(p + (size_t)(w < 2 ? 64 : 0) * (NH * DH * 2));
  }
  SB();
  Rows F1, X;
  {
    const __amdgpu_buffer_rsrc_t rg = __builtin_amdgcn_make_buffer_rsrc((void*)(f1m ? wu : wg), 0, D * FF * 2, 0x00020000);
    load_rows(F1, rg, (unsigned)(((f1r * 36 + r36) * FF + f1c * 576) * 2), FF * 2, n36, lane);
    const __amdgpu_buffer_rsrc_t rx = __builtin_amdgcn_make_buffer_rsrc((void*)(is_wo ? wo : wd), 0, (is_wo ? NH * DH : FF) * D * 2, 0x00020000);
    const int row0 = is_wo ? head * DH + quarter * 32 + 4 * w : fidx * 36 + r36;
    load_rows(X, rx, (unsigned)(row0 * (D * 2)), D * 2, is_wo ? 4 : n36, lane);
  }
  const size_t kvoff = ((size_t)head * SK + (size_t)sp * TOK_WG) * (DH * 2);
  const __amdgpu_buffer_rsrc_t rk = __builtin_amdgcn_make_buffer_rsrc((void*)(reinterpret_cast<const char*>(kc) + kvoff), 0, TOK_WG * DH * 2, 0x00020000);
  const __amdgpu_buffer_rsrc_t rv = __builtin_amdgcn_make_buffer_rsrc((void*)(reinterpret_cast<const char*>(vc) + kvoff), 0, TOK_WG * DH * 2, 0x00020000);
  const unsigned l16 = lane * 16;
  if (tid == 0) reinterpret_cast<unsigned*>(lds)[L_CTR] = 16u;
  SB();

  {
    float xf[10];
#pragma unroll
    for (int i = 0; i < 5; ++i) { xf[2 * i] = bflo(xv[i]); xf[2 * i + 1] = bfhi(xv[i]); }
    const bool tl = lane < 32;
    float s = 0.f;
#pragma unroll
    for (int i = 0; i < 8; ++i) s += xf[i];
    s += tl ? (xf[8] + xf[9]) : 0.f;
    const float mean = wave_sum(s) * (1.f / D);
    float v = 0.f;
#pragma unroll
    for (int i = 0; i < 8; ++i) { const float d = xf[i] - mean; v = fmaf(d, d, v); }
    { const float d0 = xf[8] - mean, d1 = xf[9] - mean; v += tl ? (d0 * d0 + d1 * d1) : 0.f; }
    const float var = wave_sum(v) * (1.f / D);
    const float mb = rbf(mean), den = rbf(sqrtf(rbf(rbf(var) + 1e-5f)));
#pragma unroll
    for (int i = 0; i < 5; ++i) {
      if (i < 4 || tl) {
        const int e = 2 * (lane + 64 * i);
        *reinterpret_cast<f32x2*>(&lds[L_XN + e]) = f32x2{rbf(rbf(xf[2 * i] - mb) / den), rbf(rbf(xf[2 * i + 1] - mb) / den)};
        *reinterpret_cast<f32x2*>(&lds[L_XR + e]) = f32x2{xf[2 * i], xf[2 * i + 1]};
      }
    }
  }
  STAMP(7);
  const unsigned tag = (unsigned)ep + 1u;
  {
    float a[8];
#pragma unroll
    for (int k = 0; k < 8; ++k) a[k] = 0.f;
    const float* xn = &lds[L_XN + 72 * sp + (tid >> 4)];
    fma8(a, xn[0], WQ[0]); fma8(a, xn[32], WQ[1]);
    fma8(a, w < 2 ? xn[64] : 0.f, WQ[2]);
#pragma unroll
    for (int k = 0; k < 8; ++k) a[k] = xrow_sum(a[k]);
    if (r == 0) {
      *reinterpret_cast<f32x4*>(&lds[L_QP + w * 128 + 8 * c]) = f32x4{a[0], a[1], a[2], a[3]};
      *reinterpret_cast<f32x4*>(&lds[L_QP + w * 128 + 8 * c + 4]) = f32x4{a[4], a[5], a[6], a[7]};
    }
    __syncthreads();
    if (w == 0) {
      f32x2 s = {0.f, 0.f};
#pragma unroll
      for (int j = 0; j < 8; ++j) s += *reinterpret_cast<const f32x2*>(&lds[L_QP + j * 128 + 2 * lane]);
      __builtin_amdgcn_raw_buffer_store_b128(u32x4{__float_as_uint(s[0]), tag, __float_as_uint(s[1]), tag}, rs,
                                             WS_IN0 + (unsigned)((head * 8 + sp) * 1024 + lane * 16), 0, 16);
    }
    __builtin_amdgcn_s_barrier();
  }
  STAMP(8);
  u32x4 KA[8], VA[8], KB[8], VB[8];
  {
    const unsigned o = l16 + (w != 0 ? w : 64) * 8192;
    SB(); load_tile(KA, rk, o); SB(); load_tile(VA, rv, o); SB();
  }
  auto rows_to_lds = [&](const Rows& R, const float (&cf)[5], float crem) {
    float pa[8], pr[8];
#pragma unroll
    for (int k = 0; k < 8; ++k) { pa[k] = 0.f; pr[k] = 0.f; }
#pragma unroll
    for (int i = 0; i < 5; ++i) fma8(pa, cf[i], R.m[i]);
    fma8(pr, crem, R.rem);
#pragma unroll
    for (int k = 0; k < 8; ++k) { pr[k] += dpp<XOR8>(pr[k]); pr[k] = xrow_sum(pr[k]); }
    float* red = &lds[L_RED + w * 576];
    *reinterpret_cast<f32x4*>(red + 8 * lane) = f32x4{pa[0], pa[1], pa[2], pa[3]};
    *reinterpret_cast<f32x4*>(red + 8 * lane + 4) = f32x4{pa[4], pa[5], pa[6], pa[7]};
    if (lane < 8) {
      *reinterpret_cast<f32x4*>(red + 512 + 8 * lane) = f32x4{pr[0], pr[1], pr[2], pr[3]};
      *reinterpret_cast<f32x4*>(red + 512 + 8 * lane + 4) = f32x4{pr[4], pr[5], pr[6], pr[7]};
    }
  };
  auto red_sum = [&]() -> u32x4 {
    f32x2 s = {0.f, 0.f};
#pragma unroll
    for (int j = 0; j < 8; ++j) s += *reinterpret_cast<const f32x2*>(&lds[L_RED + j * 576 + 2 * tid]);
    return u32x4{__float_as_uint(s[0]), tag, __float_as_uint(s[1]), tag};
  };
  {
    const float* xr = &lds[L_XN + f1r * 36 + r36];
    float cf[5];
#pragma unroll
    for (int i = 0; i < 5; ++i) cf[i] = xr[i < n36 ? i : 0];
    rows_to_lds(F1, cf, xr[(lane >> 3) < n36 ? (lane >> 3) : 0]);
  }
  __syncthreads();
  if (tid < 288) {
    const int col = 2 * tid, cons = f1c * 16 + col / 36;
    const unsigned off = WS_INF + (unsigned)(cons * 9216 + (f1m * 16 + f1r) * 288 + (col % 36) * 8);
    __builtin_amdgcn_raw_buffer_store_b128(red_sum(), rs, off, 0, 16);
  }
  STAMP(9);
  unsigned q[4];
  {
    if (w == 0) {
      const unsigned char* pq = ws + WS_IN0 + head * 8192 + lane * 16;
      u32x4 g0, g1, g2, g3, g4, g5, g6, g7;
      for (unsigned spins = 0;; ++spins) {
        asm volatile("global_load_dwordx4 %0, %8, off sc1\n\t"
                     "global_load_dwordx4 %1, %8, off offset:1024 sc1\n\t"
                     "global_load_dwordx4 %2, %8, off offset:2048 sc1\n\t"
                     "global_load_dwordx4 %3, %8, off offset:3072 sc1\n\t"
                     "global_load_dwordx4 %4, %9, off sc1\n\t"
                     "global_load_dwordx4 %5, %9, off offset:1024 sc1\n\t"
                     "global_load_dwordx4 %6, %9, off offset:2048 sc1\n\t"
                     "global_load_dwordx4 %7, %9, off offset:3072 sc1\n\t"
                     "s_waitcnt vmcnt(0)"
                     : "=&v"(g0), "=&v"(g1), "=&v"(g2), "=&v"(g3), "=&v"(g4), "=&v"(g5), "=&v"(g6), "=&v"(g7)
                     : "v"(pq), "v"(pq + 4096) : "memory");
        const bool ok = g0[1] == tag && g0[3] == tag && g1[1] == tag && g1[3] == tag && g2[1] == tag && g2[3] == tag &&
                        g3[1] == tag && g3[3] == tag && g4[1] == tag && g4[3] == tag && g5[1] == tag && g5[3] == tag &&
                        g6[1] == tag && g6[3] == tag && g7[1] == tag && g7[3] == tag;
        if (__all(ok) || spins > SPIN_CAP) break;
        __builtin_amdgcn_s_sleep(1);
      }
      const float q0 = ((__uint_as_float(g0[0]) + __uint_as_float(g1[0])) + (__uint_as_float(g2[0]) + __uint_as_float(g3[0]))) +
                       ((__uint_as_float(g4[0]) + __uint_as_float(g5[0])) + (__uint_as_float(g6[0]) + __uint_as_float(g7[0])));
      const float q1 = ((__uint_as_float(g0[2]) + __uint_as_float(g1[2])) + (__uint_as_float(g2[2]) + __uint_as_float(g3[2]))) +
                       ((__uint_as_float(g4[2]) + __uint_as_float(g5[2])) + (__uint_as_float(g6[2]) + __uint_as_float(g7[2])));
      reinterpret_cast<unsigned*>(lds)[L_QS + lane] = packbf(q0, q1);
    }
    __syncthreads();
    if (w == 0) {
      SB(); load_tile(KA, rk, l16); SB(); load_tile(VA, rv, l16); SB();
    }
    SB(); load_tile(KB, rk, l16 + (8 + w) * 8192); SB(); load_tile(VB, rv, l16 + (8 + w) * 8192);
    SB();
    const u32x4 qq = *reinterpret_cast<const u32x4*>(&lds[L_QS + 4 * c]);
    q[0] = qq[0]; q[1] = qq[1]; q[2] = qq[2]; q[3] = qq[3];
  }

  STAMP(1);
  State st; st.m = -3.0e38f; st.l = 0.f;
#pragma unroll
  for (int k = 0; k < 8; ++k) st.acc[k] = 0.f;
  auto grab = [&]() -> int {
    unsigned t = 0;
    if (lane == 0) t = __hip_atomic_fetch_add(&reinterpret_cast<unsigned*>(lds)[L_CTR], 1u, __ATOMIC_RELAXED, __HIP_MEMORY_SCOPE_WORKGROUP);
    return __builtin_amdgcn_readfirstlane(t);
  };
  int tA = 0, tB = 0;
  do {
    const int tA2 = grab();
    float p = qk_softmax(KA, q, st, c, true); SB();
    load_tile(KA, rk, l16 + tA2 * 8192); SB();
    pv(VA, p, st); SB();
    load_tile(VA, rv, l16 + tA2 * 8192); SB();
    const int tB2 = grab();
    p = qk_softmax(KB, q, st, c, tB < NTILE); SB();
    load_tile(KB, rk, l16 + tB2 * 8192); SB();
    pv(VB, p, st); SB();
    load_tile(VB, rv, l16 + tB2 * 8192); SB();
    tA = tA2; tB = tB2;
  } while (tA < NTILE);
  STAMP(2);

  {
    const float mw = xrow_max(st.m);
    const float f = __builtin_amdgcn_exp2f((st.m - mw) * L2E);
    const float lw = xrow_sum(row_sum(st.l) * (0.5f * f));
#pragma unroll
    for (int k = 0; k < 8; ++k) st.acc[k] = xrow_sum(st.acc[k] * f);
    float* rec = &lds[L_MRG + w * 132];
    if (r == 0) {
      *reinterpret_cast<f32x4*>(rec + 4 + 8 * c) = f32x4{st.acc[0], st.acc[1], st.acc[2], st.acc[3]};
      *reinterpret_cast<f32x4*>(rec + 8 + 8 * c) = f32x4{st.acc[4], st.acc[5], st.acc[6], st.acc[7]};
      if (c == 0) { rec[0] = mw; rec[1] = lw; }
    }
    __syncthreads();
    if (tid < 136) {
      float M = lds[L_MRG];
#pragma unroll
      for (int j = 1; j < 8; ++j) M = fmaxf(M, lds[L_MRG + j * 132]);
      const int src = tid < 128 ? 4 + tid : 1;
      float v = 0.f;
#pragma unroll
      for (int j = 0; j < 8; ++j) v = fmaf(__builtin_amdgcn_exp2f((lds[L_MRG + j * 132] - M) * L2E), lds[L_MRG + j * 132 + src], v);
      int qt, idx;
      if (tid < 128) { qt = tid >> 5; idx = tid & 31; }
      else { qt = (tid - 128) >> 1; idx = 32 + (tid & 1); if (!(tid & 1)) v = M; }
      const unsigned off = WS_IN1 + (unsigned)(((head * 4 + qt) * 8 + sp) * 512 + idx * 8);
      __builtin_amdgcn_raw_buffer_store_b64(u32x2{__float_as_uint(v), tag}, rs, off, 0, 16);
    }
  }
  STAMP(3);

  float cf[5], crem;
  if (is_wo) {
    {
      const unsigned off = WS_IN1 + (unsigned)(((head * 4 + quarter) * 8 + w) * 512 + (lane < 17 ? lane : 16) * 16);
      u32x4 gr;
      for (unsigned spins = 0;; ++spins) {
        gr = poll16(ws + off);
        const bool ok = gr[1] == tag && gr[3] == tag;
        if (__all(ok) || spins > SPIN_CAP) break;
        __builtin_amdgcn_s_sleep(1);
      }
      if (lane < 17) *reinterpret_cast<f32x2*>(&lds[L_REC + w * 36 + 2 * lane]) = f32x2{__uint_as_float(gr[0]), __uint_as_float(gr[2])};
    }
    __syncthreads();
    float M = lds[L_REC + 32];
#pragma unroll
    for (int j = 1; j < 8; ++j) M = fmaxf(M, lds[L_REC + j * 36 + 32]);
    const int js = lane >> 3;
    const float wt = __builtin_amdgcn_exp2f((lds[L_REC + js * 36 + 32] - M) * L2E);
    float o = wt * lds[L_REC + js * 36 + 4 * w + (lane & 3)];
    float L = wt * lds[L_REC + js * 36 + 33];
    o += dpp<XOR8>(o); L += dpp<XOR8>(L);
    o = xrow_sum(o); L = xrow_sum(L);
    o = rbf(o / L);
#pragma unroll
    for (int i = 0; i < 4; ++i) cf[i] = rdlane(o, i);
    cf[4] = 0.f;
    crem = __shfl(o, (lane >> 3) & 3);
  } else {
    {
      const unsigned char* pf = ws + WS_INF + fidx * 9216;
      u32x4 gr;
      for (unsigned spins = 0;; ++spins) {
        gr = poll16(pf + tid * 16);
        const bool ok = gr[1] == tag && gr[3] == tag;
        if (__all(ok) || spins > SPIN_CAP) break;
        __builtin_amdgcn_s_sleep(1);
      }
      *reinterpret_cast<f32x2*>(&lds[L_GU + 2 * tid]) = f32x2{__uint_as_float(gr[0]), __uint_as_float(gr[2])};
      if (w == 0) {
        for (unsigned spins = 0;; ++spins) {
          gr = poll16(pf + 8192 + lane * 16);
          const bool ok = gr[1] == tag && gr[3] == tag;
          if (__all(ok) || spins > SPIN_CAP) break;
          __builtin_amdgcn_s_sleep(1);
        }
        *reinterpret_cast<f32x2*>(&lds[L_GU + 1024 + 2 * lane]) = f32x2{__uint_as_float(gr[0]), __uint_as_float(gr[2])};
      }
    }
    __syncthreads();
    const int it = (lane & 7) < 5 ? (lane & 7) : 0;
    const int col = r36 + it < 36 ? r36 + it : 35;
    const float* src = &lds[L_GU + (lane >> 3) * 4 * 36 + col];
    float s = (src[0] + src[36]) + (src[72] + src[108]);
    s += dpp<XOR8>(s);
    { auto t = __builtin_amdgcn_permlane16_swap(__float_as_uint(s), __float_as_uint(s), false, false); s = __uint_as_float(t[0]) + __uint_as_float(t[1]); }
    s = rbf(s);
    const float up = __shfl(s, (lane & 7) + 32), gt = __shfl(s, lane & 7);
    const float inner = 0.7978845608028654f * (gt + 0.044715f * gt * gt * gt);
    const float ge = rbf(0.5f * gt * (1.f + tanhf(inner)));
    const float h = rbf(ge * up);
#pragma unroll
    for (int i = 0; i < 5; ++i) cf[i] = rdlane(h, i);
    crem = __shfl(h, (lane >> 3) < 5 ? (lane >> 3) : 0);
  }
  STAMP(4);
  rows_to_lds(X, cf, crem);
  __syncthreads();
  if (tid < 288) {
    const int slot = is_wo ? head * 4 + quarter : 128 + fidx;
    const unsigned off = WS_IN2 + (unsigned)((tid >> 1) * 8192 + slot * 32 + (tid & 1) * 16);
    __builtin_amdgcn_raw_buffer_store_b128(red_sum(), rs, off, 0, 16);
  }
  STAMP(5);

  if (b < NRED) {
    const int R = b;
    const unsigned off = WS_IN2 + (unsigned)(R * 8192 + tid * 16);
    u32x4 gr;
    for (unsigned spins = 0;; ++spins) {
      gr = poll16(ws + off);
      const bool ok = gr[1] == tag && gr[3] == tag;
      if (__all(ok) || spins > SPIN_CAP) break;
      __builtin_amdgcn_s_sleep(1);
    }
    float v0 = __uint_as_float(gr[0]), v1 = __uint_as_float(gr[2]);
    v0 += dpp<ROR2>(v0); v1 += dpp<ROR2>(v1);
    v0 += dpp<ROR4>(v0); v1 += dpp<ROR4>(v1);
    v0 += dpp<XOR8>(v0); v1 += dpp<XOR8>(v1);
    v0 = xrow_sum(v0); v1 = xrow_sum(v1);
    if (lane < 2) *reinterpret_cast<f32x2*>(&lds[L_FIN + w * 4 + 2 * lane]) = f32x2{v0, v1};
    __syncthreads();
    if (tid < 4) {
      const float A = (lds[L_FIN + tid] + lds[L_FIN + 4 + tid]) + (lds[L_FIN + 8 + tid] + lds[L_FIN + 12 + tid]);
      const float B = (lds[L_FIN + 16 + tid] + lds[L_FIN + 20 + tid]) + (lds[L_FIN + 24 + tid] + lds[L_FIN + 28 + tid]);
      const float o = rbf(lds[L_XR + 4 * R + tid] + rbf(A)) + rbf(B);
      out[4 * R + tid] = __builtin_bit_cast(u16, (__bf16)o);
    }
    if (R == 0 && tid == 0)
      __hip_atomic_store(reinterpret_cast<u64*>(ws + WS_E), ep + 1ull, __ATOMIC_RELAXED, __HIP_MEMORY_SCOPE_AGENT);
  }
  STAMP(6);
}

}

extern "C" void kernel_launch(void* const* d_in, const int* in_sizes, int n_in,
                              void* d_out, int out_size, void* d_ws, size_t ws_size,
                              hipStream_t stream) {
  fused_decode<<<NWG, NTHR, 0, stream>>>(
      (const u16*)d_in[0], (const u16*)d_in[1], (const u16*)d_in[2], (const u16*)d_in[3], (const u16*)d_in[4],
      (const u16*)d_in[6], (const u16*)d_in[7], (const u16*)d_in[8], (u16*)d_out, (unsigned char*)d_ws);
}
```

```cpp
#include <hip/hip_runtime.h>
#include <hip/hip_bf16.h>
#include <stdint.h>

namespace {

typedef unsigned u32x4 __attribute__((ext_vector_type(4)));
typedef unsigned u32x2 __attribute__((ext_vector_type(2)));
typedef float f32x4 __attribute__((ext_vector_type(4)));
typedef float f32x2 __attribute__((ext_vector_type(2)));
typedef __bf16 bf16x2_t __attribute__((ext_vector_type(2)));
typedef unsigned short u16;
typedef unsigned long long u64;

constexpr int D = 576, NH = 32, SK = 16384, DH = 128, FF = 4608;
constexpr int NWG = 256, NTHR = 512;
constexpr int T = 4;
constexpr unsigned TB = T * 1024;
constexpr int NTILE = SK * DH * 2 / TB;
#define FETCH_N 4
#define NX 2
constexpr float L2E = 1.4426950408889634f;
constexpr float SCALE = 0.08838834764831845f;
constexpr unsigned OOB = 0x40000000u;

constexpr unsigned WS_E = 0;
constexpr unsigned WS_IND = 4096;
constexpr unsigned WS_IN0 = 8192;
constexpr unsigned WS_REC = WS_IN0 + 32 * 8 * 1024;
constexpr unsigned WS_IN2 = WS_REC + 32 * 8 * 1024;
constexpr unsigned WS_END = WS_IN2 + 144 * 4096;
constexpr unsigned SPIN_CAP = 1u << 18;

constexpr int L_XN = 0;
constexpr int L_XR = L_XN + 576;
constexpr int L_QP = L_XR + 576;
constexpr int L_MRG = L_QP + 8 * 128;
constexpr int L_H = L_MRG + 8 * 132;
constexpr int L_RED = L_H + 64;
constexpr int L_O = L_RED + 8 * 576;
constexpr int L_FIN = L_O + 8 * 128;
constexpr int L_QS = L_FIN + 544;
constexpr int L_CTR = L_QS + 64;
constexpr int L_TOTAL = L_CTR + 4;

__device__ __forceinline__ float bflo(unsigned u) { return __uint_as_float(u << 16); }
__device__ __forceinline__ float bfhi(unsigned u) { return __uint_as_float(u & 0xffff0000u); }
__device__ __forceinline__ float rbf(float x) { return (float)(__bf16)x; }
__device__ __forceinline__ unsigned packbf(float lo, float hi) {
  bf16x2_t v; v[0] = (__bf16)lo; v[1] = (__bf16)hi; return __builtin_bit_cast(unsigned, v);
}
__device__ __forceinline__ float dot2(unsigned a, unsigned b, float acc) {
  return __builtin_amdgcn_fdot2_f32_bf16(__builtin_bit_cast(bf16x2_t, a), __builtin_bit_cast(bf16x2_t, b), acc, false);
}
template <int CTRL> __device__ __forceinline__ float dpp(float x) {
  return __builtin_bit_cast(float, __builtin_amdgcn_mov_dpp(__builtin_bit_cast(int, x), CTRL, 0xf, 0xf, true));
}
constexpr int XOR1 = 0xB1, XOR2 = 0x4E, XOR7 = 0x141, XOR8 = 0x128, ROR2 = 0x122, ROR4 = 0x124;
__device__ __forceinline__ float xrow_max(float x) {
  auto s = __builtin_amdgcn_permlane16_swap(__float_as_uint(x), __float_as_uint(x), false, false);
  x = fmaxf(__uint_as_float(s[0]), __uint_as_float(s[1]));
  auto t = __builtin_amdgcn_permlane32_swap(__float_as_uint(x), __float_as_uint(x), false, false);
  return fmaxf(__uint_as_float(t[0]), __uint_as_float(t[1]));
}
__device__ __forceinline__ float xrow_sum(float x) {
  auto s = __builtin_amdgcn_permlane16_swap(__float_as_uint(x), __float_as_uint(x), false, false);
  x = __uint_as_float(s[0]) + __uint_as_float(s[1]);
  auto t = __builtin_amdgcn_permlane32_swap(__float_as_uint(x), __float_as_uint(x), false, false);
  return __uint_as_float(t[0]) + __uint_as_float(t[1]);
}
__device__ __forceinline__ f32x2 xrow_sum8(const float (&v)[8]) {
  float t[4];
#pragma unroll
  for (int j = 0; j < 4; ++j) {
    const int a = (j >> 1) * 4 + (j & 1);
    auto s = __builtin_amdgcn_permlane16_swap(__float_as_uint(v[a]), __float_as_uint(v[a + 2]), false, false);
    t[j] = __uint_as_float(s[0]) + __uint_as_float(s[1]);
  }
  auto s0 = __builtin_amdgcn_permlane32_swap(__float_as_uint(t[0]), __float_as_uint(t[2]), false, false);
  auto s1 = __builtin_amdgcn_permlane32_swap(__float_as_uint(t[1]), __float_as_uint(t[3]), false, false);
  return f32x2{__uint_as_float(s0[0]) + __uint_as_float(s0[1]), __uint_as_float(s1[0]) + __uint_as_float(s1[1])};
}
__device__ __forceinline__ float row_sum(float x) {
  x += dpp<XOR1>(x); x += dpp<XOR2>(x); x += dpp<XOR7>(x); x += dpp<XOR8>(x); return x;
}
__device__ __forceinline__ float wave_sum(float x) { return xrow_sum(row_sum(x)); }
__device__ __forceinline__ u32x4 ldnt(const void* p) { return __builtin_nontemporal_load(reinterpret_cast<const u32x4*>(p)); }
__device__ __forceinline__ float rdlane(float x, int l) { return __builtin_bit_cast(float, __builtin_amdgcn_readlane(__builtin_bit_cast(int, x), l)); }
__device__ __forceinline__ float u2f(unsigned u) { return __uint_as_float(u); }
#define SB() __builtin_amdgcn_sched_barrier(0)
#define STAMP(i)
__device__ __forceinline__ u32x4 poll16(const unsigned char* p) {
  u32x4 v;
  asm volatile("global_load_dwordx4 %0, %1, off sc1\n\ts_waitcnt vmcnt(0)" : "=&v"(v) : "v"(p) : "memory");
  return v;
}
__device__ __forceinline__ bool tagged(const u32x4& g, unsigned tag) { return g[1] == tag && g[3] == tag; }

struct State { float m, l; float acc[8]; };

__device__ __forceinline__ float qk_softmax(const u32x4 (&K)[T], const unsigned (&q)[4], State& st, int c) {
  float x[4];
#pragma unroll
  for (int i = 0; i < 4; ++i) {
    float d = dot2(K[i][0], q[0], 0.f);
    d = dot2(K[i][1], q[1], d); d = dot2(K[i][2], q[2], d); d = dot2(K[i][3], q[3], d);
    x[i] = d;
  }
  float s, y[2];
#pragma unroll
  for (int j = 0; j < 2; ++j) { const float a = x[j] + dpp<XOR8>(x[j]), b = x[j + 2] + dpp<XOR8>(x[j + 2]); y[j] = (c & 8) ? b : a; }
  { const float a = y[0] + dpp<XOR7>(y[0]), b = y[1] + dpp<XOR7>(y[1]); s = (c & 4) ? b : a; }
  s += dpp<XOR2>(s);
  s += dpp<XOR1>(s);
  s = rbf(rbf(s) * SCALE);
  float tm = fmaxf(s, dpp<XOR7>(s)); tm = fmaxf(tm, dpp<XOR8>(tm));
  const float mn = fmaxf(st.m, tm);
  const float alpha = __builtin_amdgcn_exp2f((st.m - mn) * L2E);
  const float p = __builtin_amdgcn_exp2f((s - mn) * L2E);
  st.l = fmaf(st.l, alpha, p); st.m = mn;
#pragma unroll
  for (int k = 0; k < 8; ++k) st.acc[k] *= alpha;
  return p;
}
template <int I> __device__ __forceinline__ void pv_one(const u32x4& V, float p, State& st) {
  const float pb = dpp<0x150 + 4 * I>(p);
#pragma unroll
  for (int k = 0; k < 4; ++k) {
    st.acc[2 * k] = fmaf(pb, bflo(V[k]), st.acc[2 * k]);
    st.acc[2 * k + 1] = fmaf(pb, bfhi(V[k]), st.acc[2 * k + 1]);
  }
}
__device__ __forceinline__ void pv(const u32x4 (&V)[T], float p, State& st) {
  pv_one<0>(V[0], p, st); pv_one<1>(V[1], p, st); pv_one<2>(V[2], p, st); pv_one<3>(V[3], p, st);
  asm volatile("" : "+v"(st.acc[0]), "+v"(st.acc[1]), "+v"(st.acc[2]), "+v"(st.acc[3]),
                    "+v"(st.acc[4]), "+v"(st.acc[5]), "+v"(st.acc[6]), "+v"(st.acc[7]), "+v"(st.l));
}
__device__ __forceinline__ void load_tile(u32x4 (&B)[T], __amdgpu_buffer_rsrc_t rsrc, unsigned voff) {
#pragma unroll
  for (int i = 0; i < T; ++i) B[i] = __builtin_amdgcn_raw_buffer_load_b128(rsrc, voff + i * 1024, 0, 2);
}
__device__ __forceinline__ void fma8(float (&a)[8], float s, const u32x4& w) {
#pragma unroll
  for (int k = 0; k < 4; ++k) { a[2 * k] = fmaf(s, bflo(w[k]), a[2 * k]); a[2 * k + 1] = fmaf(s, bfhi(w[k]), a[2 * k + 1]); }
}

__device__ __forceinline__ void fma8b(float (&a)[8], float s, const u32x4& w) {
  const unsigned hi = __float_as_uint(s) & 0xffff0000u, lo = hi >> 16;
#pragma unroll
  for (int k = 0; k < 4; ++k) { a[2 * k] = dot2(w[k], lo, a[2 * k]); a[2 * k + 1] = dot2(w[k], hi, a[2 * k + 1]); }
}

__global__ void __launch_bounds__(NTHR, 2)
fused_decode(const u16* __restrict__ x, const u16* __restrict__ kc, const u16* __restrict__ vc,
             const u16* __restrict__ wq, const u16* __restrict__ wo, const u16* __restrict__ wg,
             const u16* __restrict__ wu, const u16* __restrict__ wd, u16* __restrict__ out,
             unsigned char* ws) {
  __shared__ __attribute__((aligned(16))) float lds[L_TOTAL];
  const int tid = threadIdx.x, lane = tid & 63, w = __builtin_amdgcn_readfirstlane(tid >> 6);
  const int r = lane >> 4, c = lane & 15;
  const int b = blockIdx.x, g = b & 7, bi = b >> 3;
  const bool attn = bi < 24;
  const int head = g * 4 + (attn ? bi / 6 : (bi - 24) >> 1);
  const int sp = attn ? bi % 6 : 6 + (bi & 1);
  const __amdgpu_buffer_rsrc_t rs = __builtin_amdgcn_make_buffer_rsrc((void*)ws, 0, (int)WS_END, 0x00020000);
  const unsigned l16 = lane * 16;
  const int tile0 = (NTILE * sp) / 6, ntl = (NTILE * (sp + 1)) / 6 - tile0;

  STAMP(0);
  unsigned xv[5]; u64 ep; unsigned tag; u32x4 WQ[3];
  const size_t kvoff = (size_t)head * SK * (DH * 2);
  const __amdgpu_buffer_rsrc_t rk = __builtin_amdgcn_make_buffer_rsrc((void*)(reinterpret_cast<const char*>(kc) + kvoff), 0, SK * DH * 2, 0x00020000);
  const __amdgpu_buffer_rsrc_t rv = __builtin_amdgcn_make_buffer_rsrc((void*)(reinterpret_cast<const char*>(vc) + kvoff), 0, SK * DH * 2, 0x00020000);
  auto pro1 = [&]() {
  const unsigned* x32 = reinterpret_cast<const unsigned*>(x);
#pragma unroll
  for (int i = 0; i < 4; ++i) xv[i] = x32[lane + 64 * i];
  xv[4] = x32[256 + (lane & 31)];
  ep = __hip_atomic_load(reinterpret_cast<const u64*>(ws + WS_E), __ATOMIC_RELAXED, __HIP_MEMORY_SCOPE_AGENT);
  {
    const char* p = reinterpret_cast<const char*>(wq) + ((size_t)(72 * sp + (tid >> 4)) * (NH * DH) + head * DH + 8 * c) * 2;
    WQ[0] = ldnt(p); WQ[1] = ldnt(p + (size_t)32 * (NH * DH * 2));
    WQ[2] = ldnt(p + (size_t)(w < 2 ? 64 : 0) * (NH * DH * 2));
  }
  if (tid == 0) reinterpret_cast<unsigned*>(lds)[L_CTR] = 16u + 8u * NX;
  SB();
  };
  auto pro2 = [&]() {
  {
    float xf[10];
    asm volatile("" : "+v"(xv[0]), "+v"(xv[1]), "+v"(xv[2]), "+v"(xv[3]), "+v"(xv[4]));
#pragma unroll
    for (int i = 0; i < 5; ++i) { xf[2 * i] = bflo(xv[i]); xf[2 * i + 1] = bfhi(xv[i]); }
    const bool tl = lane < 32;
    float s = 0.f;
#pragma unroll
    for (int i = 0; i < 8; ++i) s += xf[i];
    s += tl ? (xf[8] + xf[9]) : 0.f;
    const float mean = wave_sum(s) * (1.f / D);
    float v = 0.f;
#pragma unroll
    for (int i = 0; i < 8; ++i) { const float d = xf[i] - mean; v = fmaf(d, d, v); }
    { const float d0 = xf[8] - mean, d1 = xf[9] - mean; v += tl ? (d0 * d0 + d1 * d1) : 0.f; }
    const float var = wave_sum(v) * (1.f / D);
    const float mb = rbf(mean), den = rbf(sqrtf(rbf(rbf(var) + 1e-5f)));
#pragma unroll
    for (int i = 0; i < 5; ++i) {
      if (i < 4 || tl) {
        const int e = 2 * (lane + 64 * i);
        *reinterpret_cast<f32x2*>(&lds[L_XN + e]) = f32x2{rbf(rbf(xf[2 * i] - mb) / den), rbf(rbf(xf[2 * i + 1] - mb) / den)};
        *reinterpret_cast<f32x2*>(&lds[L_XR + e]) = f32x2{xf[2 * i], xf[2 * i + 1]};
      }
    }
  }
  STAMP(7);
  tag = (unsigned)ep; asm volatile("" : "+v"(tag)); tag += 1u;
  {
    float a[8];
#pragma unroll
    for (int k = 0; k < 8; ++k) a[k] = 0.f;
    const float* xn = &lds[L_XN + 72 * sp + (tid >> 4)];
    fma8(a, xn[0], WQ[0]); fma8(a, xn[32], WQ[1]);
    fma8(a, w < 2 ? xn[64] : 0.f, WQ[2]);
#pragma unroll
    for (int k = 0; k < 8; ++k) a[k] = xrow_sum(a[k]);
    if (r == 0) {
      *reinterpret_cast<f32x4*>(&lds[L_QP + w * 128 + 8 * c]) = f32x4{a[0], a[1], a[2], a[3]};
      *reinterpret_cast<f32x4*>(&lds[L_QP + w * 128 + 8 * c + 4]) = f32x4{a[4], a[5], a[6], a[7]};
    }
    __syncthreads();
    if (w == 0) {
      f32x2 s = {0.f, 0.f};
#pragma unroll
      for (int j = 0; j < 8; ++j) s += *reinterpret_cast<const f32x2*>(&lds[L_QP + j * 128 + 2 * lane]);
      __builtin_amdgcn_raw_buffer_store_b128(u32x4{__float_as_uint(s[0]), tag, __float_as_uint(s[1]), tag}, rs,
                                             WS_IN0 + (unsigned)((head * 8 + sp) * 1024 + lane * 16), 0, 16);
    }
    __syncthreads();
  }
  STAMP(8);

  };

  if (attn) {
    u32x4 KA[T], VA[T], KX[NX][T], VX[NX][T];
    asm volatile("; attention path");
    pro1();
    __builtin_amdgcn_s_barrier();
    { const unsigned o = w != 0 ? l16 + (tile0 + w) * TB : OOB; SB(); load_tile(KA, rk, o); SB(); load_tile(VA, rv, o); SB(); }
    pro2();
    unsigned q[4];
    if (w != 0) {
#pragma unroll
      for (int j = 0; j < NX; ++j) { SB(); load_tile(KX[j], rk, l16 + (tile0 + 8 * j + 8 + w) * TB); SB(); load_tile(VX[j], rv, l16 + (tile0 + 8 * j + 8 + w) * TB); SB(); }
    } else {
      const unsigned char* pq = ws + WS_IN0 + head * 8192 + lane * 16;
      u32x4 g0, g1, g2, g3, g4, g5, g6, g7;
      for (unsigned spins = 0;; ++spins) {
        asm volatile("global_load_dwordx4 %0, %8, off sc1\n\t"
                     "global_load_dwordx4 %1, %8, off offset:1024 sc1\n\t"
                     "global_load_dwordx4 %2, %8, off offset:2048 sc1\n\t"
                     "global_load_dwordx4 %3, %8, off offset:3072 sc1\n\t"
                     "global_load_dwordx4 %4, %9, off sc1\n\t"
                     "global_load_dwordx4 %5, %9, off offset:1024 sc1\n\t"
                     "global_load_dwordx4 %6, %9, off offset:2048 sc1\n\t"
                     "global_load_dwordx4 %7, %9, off offset:3072 sc1\n\t"
                     "s_waitcnt vmcnt(0)"
                     : "=&v"(g0), "=&v"(g1), "=&v"(g2), "=&v"(g3), "=&v"(g4), "=&v"(g5), "=&v"(g6), "=&v"(g7)
                     : "v"(pq), "v"(pq + 4096) : "memory");
        const bool ok = tagged(g0, tag) && tagged(g1, tag) && tagged(g2, tag) && tagged(g3, tag) &&
                        tagged(g4, tag) && tagged(g5, tag) && tagged(g6, tag) && tagged(g7, tag);
        if (__all(ok) || spins > SPIN_CAP) break;
        __builtin_amdgcn_s_sleep(1);
      }
      const float q0 = ((u2f(g0[0]) + u2f(g1[0])) + (u2f(g2[0]) + u2f(g3[0]))) + ((u2f(g4[0]) + u2f(g5[0])) + (u2f(g6[0]) + u2f(g7[0])));
      const float q1 = ((u2f(g0[2]) + u2f(g1[2])) + (u2f(g2[2]) + u2f(g3[2]))) + ((u2f(g4[2]) + u2f(g5[2])) + (u2f(g6[2]) + u2f(g7[2])));
      reinterpret_cast<unsigned*>(lds)[L_QS + lane] = packbf(q0, q1);
    }
    __syncthreads();
    if (w == 0) {
      SB(); load_tile(KA, rk, l16 + tile0 * TB); SB(); load_tile(VA, rv, l16 + tile0 * TB); SB();
#pragma unroll
      for (int j = 0; j < NX; ++j) { SB(); load_tile(KX[j], rk, l16 + (tile0 + 8 * j + 8) * TB); SB(); load_tile(VX[j], rv, l16 + (tile0 + 8 * j + 8) * TB); SB(); }
    }
    SB();
    {
      const u32x4 qq = *reinterpret_cast<const u32x4*>(&lds[L_QS + 4 * c]);
      q[0] = qq[0]; q[1] = qq[1]; q[2] = qq[2]; q[3] = qq[3];
    }
    STAMP(1);
    State st; st.m = -3.0e38f; st.l = 0.f;
#pragma unroll
    for (int k = 0; k < 8; ++k) st.acc[k] = 0.f;
    float p = qk_softmax(KA, q, st, c); SB();
    load_tile(KA, rk, l16 + (tile0 + 8 * NX + 8 + w) * TB); SB();
    pv(VA, p, st); SB();
    load_tile(VA, rv, l16 + (tile0 + 8 * NX + 8 + w) * TB); SB();
#pragma unroll
    for (int j = 0; j < NX; ++j) { p = qk_softmax(KX[j], q, st, c); SB(); pv(VX[j], p, st); SB(); }
    for (;;) {
      unsigned t = 0;
      if (lane == 0) t = __hip_atomic_fetch_add(&reinterpret_cast<unsigned*>(lds)[L_CTR], 1u, __ATOMIC_RELAXED, __HIP_MEMORY_SCOPE_WORKGROUP);
      p = qk_softmax(KA, q, st, c); SB();
      const int loc = __builtin_amdgcn_readfirstlane(t);
      if (loc >= ntl) break;
      load_tile(KA, rk, l16 + (tile0 + loc) * TB); SB();
      pv(VA, p, st); SB();
      load_tile(VA, rv, l16 + (tile0 + loc) * TB); SB();
    }
    pv(VA, p, st); SB();
    STAMP(2);
    const float mw = xrow_max(st.m);
    const float f = __builtin_amdgcn_exp2f((st.m - mw) * L2E);
    const float lw = xrow_sum(row_sum(st.l) * (0.25f * f));
#pragma unroll
    for (int k = 0; k < 8; ++k) st.acc[k] *= f;
    float* rec = &lds[L_MRG + w * 132];
    *reinterpret_cast<f32x2*>(rec + 4 + 8 * c + 2 * r) = xrow_sum8(st.acc);
    if (lane == 0) *reinterpret_cast<f32x2*>(rec) = f32x2{mw, lw};
    __syncthreads();
    if (tid < 65) {
      float M = lds[L_MRG];
#pragma unroll
      for (int j = 1; j < 8; ++j) M = fmaxf(M, lds[L_MRG + j * 132]);
      const int src = tid < 64 ? 4 + 2 * tid : 0;
      float v0 = 0.f, v1 = 0.f;
#pragma unroll
      for (int j = 0; j < 8; ++j) {
        const float wt = __builtin_amdgcn_exp2f((lds[L_MRG + j * 132] - M) * L2E);
        const f32x2 a = *reinterpret_cast<const f32x2*>(&lds[L_MRG + j * 132 + src]);
        v0 = fmaf(wt, a[0], v0); v1 = fmaf(wt, a[1], v1);
      }
      if (tid == 64) v0 = M;
      const unsigned off = tid < 64 ? WS_REC + (unsigned)((head * 8 + sp) * 1024 + tid * 16) : WS_IND + (unsigned)((head * 8 + sp) * 16);
      __builtin_amdgcn_raw_buffer_store_b128(u32x4{__float_as_uint(v0), tag, __float_as_uint(v1), tag}, rs, off, 0, 16);
    }
    STAMP(3);
    const int R = g * 18 + bi;
    if (bi < 18 && w == 7) {
      const unsigned char* pz = ws + WS_IN2 + R * 4096 + l16;
      float sum[2][2];
#pragma unroll
      for (int ph = 0; ph < 2; ++ph) {
        u32x4 g0 = {0u, 0u, 0u, 0u}, g1 = g0;
        bool ok = false;
        for (unsigned spins = 0;; ++spins) {
          if (!ok) {
            asm volatile("global_load_dwordx4 %0, %2, off sc1\n\t"
                         "global_load_dwordx4 %1, %2, off offset:1024 sc1\n\t"
                         "s_waitcnt vmcnt(0)"
                         : "+v"(g0), "+v"(g1) : "v"(pz + ph * 2048) : "memory");
            ok = tagged(g0, tag) && tagged(g1, tag);
          }
          if (__all(ok) || spins > SPIN_CAP) break;
          if (__builtin_popcountll(__ballot(!ok)) > 16) __builtin_amdgcn_s_sleep(8); else __builtin_amdgcn_s_sleep(1);
        }
        float v0 = u2f(g0[0]) + u2f(g1[0]), v1 = u2f(g0[2]) + u2f(g1[2]);
        v0 += dpp<ROR2>(v0); v1 += dpp<ROR2>(v1);
        v0 += dpp<ROR4>(v0); v1 += dpp<ROR4>(v1);
        v0 += dpp<XOR8>(v0); v1 += dpp<XOR8>(v1);
        sum[ph][0] = xrow_sum(v0); sum[ph][1] = xrow_sum(v1);
      }
      if (lane < 2) {
        const f32x2 xr = *reinterpret_cast<const f32x2*>(&lds[L_XR + 4 * R + 2 * lane]);
        const float o0 = rbf(xr[0] + rbf(sum[1][0])) + rbf(sum[0][0]);
        const float o1 = rbf(xr[1] + rbf(sum[1][1])) + rbf(sum[0][1]);
        reinterpret_cast<unsigned*>(out)[2 * R + lane] = packbf(o0, o1);
      }
      if (R == 0 && lane == 0)
        __hip_atomic_store(reinterpret_cast<u64*>(ws + WS_E), ep + 1ull, __ATOMIC_RELAXED, __HIP_MEMORY_SCOPE_AGENT);
    }
  } else {
    asm volatile("; service path");
    pro1();
    pro2();
    const int half = bi & 1, sv = head * 2 + half;
    const int blk0 = sv, nblk = (bi == 31) ? 2 : 1;
    const __amdgpu_buffer_rsrc_t rd = __builtin_amdgcn_make_buffer_rsrc((void*)wd, 0, FF * D * 2, 0x00020000);
    float pa[8], pr[8];
#pragma unroll
    for (int k = 0; k < 8; ++k) { pa[k] = 0.f; pr[k] = 0.f; }
    for (int ib = 0; ib < nblk; ++ib) {
      const int bk = ib == 0 ? blk0 : 64 + g;
      u32x4 G[18], Y[9];
      const char* pg = reinterpret_cast<const char*>(c < 8 ? wg : wu) + ((size_t)(72 * w + 18 * r) * FF + bk * 64 + (c & 7) * 8) * 2;
      SB();
#pragma unroll
      for (int i = 0; i < 18; ++i) G[i] = ldnt(pg + (size_t)i * (FF * 2));
      const unsigned od = (unsigned)((bk * 64 + 8 * w) * (D * 2));
#pragma unroll
      for (int i = 0; i < 8; ++i) Y[i] = __builtin_amdgcn_raw_buffer_load_b128(rd, od + i * (D * 2) + l16, 0, 2);
      Y[8] = __builtin_amdgcn_raw_buffer_load_b128(rd, od + (lane >> 3) * (D * 2) + 1024 + (lane & 7) * 16, 0, 2);
      SB();
      float a[8];
#pragma unroll
      for (int k = 0; k < 8; ++k) a[k] = 0.f;
      const float* xn = &lds[L_XN + 72 * w + 18 * r];
#pragma unroll
      for (int i = 0; i < 18; ++i) fma8(a, xn[i], G[i]);
#pragma unroll
      for (int k = 0; k < 8; ++k) a[k] = xrow_sum(a[k]);
      if (r == 0) {
        *reinterpret_cast<f32x4*>(&lds[L_QP + w * 128 + 8 * c]) = f32x4{a[0], a[1], a[2], a[3]};
        *reinterpret_cast<f32x4*>(&lds[L_QP + w * 128 + 8 * c + 4]) = f32x4{a[4], a[5], a[6], a[7]};
      }
      __syncthreads();
      float gt = lds[L_QP + (lane >> 3) * 128 + 8 * w + (lane & 7)], up = lds[L_QP + (lane >> 3) * 128 + 64 + 8 * w + (lane & 7)];
      gt += dpp<XOR8>(gt); up += dpp<XOR8>(up);
      gt = rbf(xrow_sum(gt)); up = rbf(xrow_sum(up));
      const float inner = 0.7978845608028654f * (gt + 0.044715f * gt * gt * gt);
      const float ge = rbf(0.5f * gt * (1.f + tanhf(inner)));
      const float h = rbf(ge * up);
#pragma unroll
      for (int i = 0; i < 8; ++i) fma8(pa, rdlane(h, i), Y[i]);
      fma8(pr, __shfl(h, lane >> 3), Y[8]);
      __syncthreads();
    }
    STAMP(1);
    u32x4 W[16];
    {
      const __amdgpu_buffer_rsrc_t ro = __builtin_amdgcn_make_buffer_rsrc((void*)wo, 0, NH * DH * D * 2, 0x00020000);
      const unsigned oo = c < 9 ? (unsigned)((sv * 64 + 16 * r) * (D * 2) + w * 144 + c * 16) : OOB;
      SB();
#pragma unroll
      for (int i = 0; i < 16; ++i) W[i] = __builtin_amdgcn_raw_buffer_load_b128(ro, oo, (unsigned)(i * (D * 2)), 0);
      SB();
    }
    {
#pragma unroll
      for (int k = 0; k < 8; ++k) { pr[k] += dpp<XOR8>(pr[k]); pr[k] = xrow_sum(pr[k]); }
      float* red = &lds[L_RED + w * 576];
      *reinterpret_cast<f32x4*>(red + 8 * lane) = f32x4{pa[0], pa[1], pa[2], pa[3]};
      *reinterpret_cast<f32x4*>(red + 8 * lane + 4) = f32x4{pa[4], pa[5], pa[6], pa[7]};
      if (lane < 8) {
        *reinterpret_cast<f32x4*>(red + 512 + 8 * lane) = f32x4{pr[0], pr[1], pr[2], pr[3]};
        *reinterpret_cast<f32x4*>(red + 512 + 8 * lane + 4) = f32x4{pr[4], pr[5], pr[6], pr[7]};
      }
      __syncthreads();
      if (tid < 288) {
        f32x2 s = {0.f, 0.f};
#pragma unroll
        for (int j = 0; j < 8; ++j) s += *reinterpret_cast<const f32x2*>(&lds[L_RED + j * 576 + 2 * tid]);
        const unsigned off = WS_IN2 + (unsigned)((tid >> 1) * 4096 + sv * 32 + (tid & 1) * 16);
        __builtin_amdgcn_raw_buffer_store_b128(u32x4{__float_as_uint(s[0]), tag, __float_as_uint(s[1]), tag}, rs, off, 0, 16);
      }
    }
#pragma unroll
    for (int j = 0; j < 8; ++j)
#pragma unroll
      for (int k = 0; k < 4; ++k) {
        const unsigned a = W[2 * j][k], bq = W[2 * j + 1][k];
        W[2 * j][k] = __builtin_amdgcn_perm(bq, a, 0x05040100u);
        W[2 * j + 1][k] = __builtin_amdgcn_perm(bq, a, 0x07060302u);
      }
#pragma unroll
    for (int i = 0; i < 16; ++i) asm volatile("" : "+v"(W[i]));
    STAMP(2);
    {
      const unsigned char* pi = ws + WS_IND + (head * 8 + (lane & 7)) * 16;
      const unsigned char* prc = ws + WS_REC + (head * 8 + (lane >> 5)) * 1024 + half * 512 + (lane & 31) * 16;
      u32x4 gi, g0, g1, g2;
      bool fetch = false;
      for (unsigned spins = 0; spins < SPIN_CAP; ++spins) {
        bool okr = false;
        if (fetch) {
          asm volatile("global_load_dwordx4 %0, %4, off sc1\n\t"
                       "global_load_dwordx4 %1, %5, off sc1\n\t"
                       "global_load_dwordx4 %2, %5, off offset:2048 sc1\n\t"
                       "global_load_dwordx4 %3, %6, off sc1\n\t"
                       "s_waitcnt vmcnt(0)"
                       : "=&v"(gi), "=&v"(g0), "=&v"(g1), "=&v"(g2) : "v"(pi), "v"(prc), "v"(prc + 4096) : "memory");
          okr = __all(tagged(g0, tag) && tagged(g1, tag) && tagged(g2, tag));
        } else {
          gi = poll16(pi);
        }
        const int n = __builtin_popcount((unsigned)__ballot(tagged(gi, tag)) & 0x3fu);
        if (okr && n == 6) break;
        fetch = n >= FETCH_N;
        if (n < 2) __builtin_amdgcn_s_sleep(32); else if (n < FETCH_N) __builtin_amdgcn_s_sleep(4); else if (n < 6) __builtin_amdgcn_s_sleep(1);
      }
      STAMP(3);
      const float ms = u2f(gi[0]), ls = u2f(gi[2]);
      float mk[6], M = -3.0e38f;
#pragma unroll
      for (int s = 0; s < 6; ++s) { mk[s] = rdlane(ms, s); M = fmaxf(M, mk[s]); }
      float L = 0.f;
#pragma unroll
      for (int s = 0; s < 6; ++s) { mk[s] = __builtin_amdgcn_exp2f((mk[s] - M) * L2E); L = fmaf(mk[s], rdlane(ls, s), L); }
      const bool up = lane >= 32;
      const float w0 = up ? mk[1] : mk[0], w1 = up ? mk[3] : mk[2], w2 = up ? mk[5] : mk[4];
      float o0 = fmaf(w2, u2f(g2[0]), fmaf(w1, u2f(g1[0]), w0 * u2f(g0[0])));
      float o1 = fmaf(w2, u2f(g2[2]), fmaf(w1, u2f(g1[2]), w0 * u2f(g0[2])));
      { auto t = __builtin_amdgcn_permlane32_swap(__float_as_uint(o0), __float_as_uint(o0), false, false); o0 = u2f(t[0]) + u2f(t[1]); }
      { auto t = __builtin_amdgcn_permlane32_swap(__float_as_uint(o1), __float_as_uint(o1), false, false); o1 = u2f(t[0]) + u2f(t[1]); }
      unsigned* ol = reinterpret_cast<unsigned*>(&lds[L_O + w * 128]);
      if (lane < 32) ol[lane] = packbf(o0 / L, o1 / L);
      const u32x4 c0 = *reinterpret_cast<const u32x4*>(ol + 8 * r), c1 = *reinterpret_cast<const u32x4*>(ol + 8 * r + 4);
      float oa[8];
#pragma unroll
      for (int k = 0; k < 8; ++k) oa[k] = 0.f;
#pragma unroll
      for (int j = 0; j < 8; ++j) {
        const unsigned cj = j < 4 ? c0[j & 3] : c1[j & 3];
#pragma unroll
        for (int k = 0; k < 4; ++k) { oa[2 * k] = dot2(W[2 * j][k], cj, oa[2 * k]); oa[2 * k + 1] = dot2(W[2 * j + 1][k], cj, oa[2 * k + 1]); }
      }
      const f32x2 sx = xrow_sum8(oa);
      const float s0 = sx[0], s1 = sx[1];
      const unsigned off = c < 9 ? WS_IN2 + (unsigned)((18 * w + 2 * c + (r >> 1)) * 4096 + 2048 + sv * 32 + (r & 1) * 16) : OOB;
      __builtin_amdgcn_raw_buffer_store_b128(u32x4{__float_as_uint(s0), tag, __float_as_uint(s1), tag}, rs, off, 0, 16);
      STAMP(4);
    }
  }
  STAMP(6);
}

}

extern "C" void kernel_launch(void* const* d_in, const int* in_sizes, int n_in,
                              void* d_out, int out_size, void* d_ws, size_t ws_size,
                              hipStream_t stream) {
  fused_decode<<<NWG, NTHR, 0, stream>>>(
      (const u16*)d_in[0], (const u16*)d_in[1], (const u16*)d_in[2], (const u16*)d_in[3], (const u16*)d_in[4],
      (const u16*)d_in[6], (const u16*)d_in[7], (const u16*)d_in[8], (u16*)d_out, (unsigned char*)d_ws);
}
```

```cpp
#include <hip/hip_runtime.h>
#include <hip/hip_bf16.h>
#include <stdint.h>

namespace {

typedef unsigned u32x4 __attribute__((ext_vector_type(4)));
typedef unsigned u32x2 __attribute__((ext_vector_type(2)));
typedef float f32x4 __attribute__((ext_vector_type(4)));
typedef float f32x2 __attribute__((ext_vector_type(2)));
typedef __bf16 bf16x2_t __attribute__((ext_vector_type(2)));
typedef unsigned short u16;
typedef unsigned long long u64;

constexpr int D = 576, NH = 32, SK = 16384, DH = 128, FF = 4608;
constexpr int NWG = 256, NTHR = 512;
constexpr int T = 4;
constexpr unsigned TB = T * 1024;
constexpr int NTILE = SK * DH * 2 / TB;
#define FETCH_N 4
#define NX 2
constexpr float L2E = 1.4426950408889634f;
constexpr float SCALE = 0.08838834764831845f;
constexpr unsigned OOB = 0x40000000u;

constexpr unsigned WS_E = 0;
constexpr unsigned WS_IND = 4096;
constexpr unsigned WS_IN0 = 8192;
constexpr unsigned WS_REC = WS_IN0 + 32 * 8 * 1024;
constexpr unsigned WS_H = WS_REC + 32 * 8 * 1024;
constexpr unsigned WS_IN2 = WS_H + FF * 8;
constexpr unsigned WS_END = WS_IN2 + 144 * 8192;
constexpr unsigned SPIN_CAP = 1u << 18;

constexpr int L_XN = 0;
constexpr int L_XR = L_XN + 576;
constexpr int L_QP = L_XR + 576;
constexpr int L_MRG = L_QP + 8 * 128;
constexpr int L_H = L_MRG + 8 * 132;
constexpr int L_RED = L_H + 64;
constexpr int L_O = L_RED + 8 * 576;
constexpr int L_FIN = L_O + 8 * 128;
constexpr int L_QS = L_FIN + 544;
constexpr int L_CTR = L_QS + 64;
constexpr int L_TOTAL = L_CTR + 4;

__device__ __forceinline__ float bflo(unsigned u) { return __uint_as_float(u << 16); }
__device__ __forceinline__ float bfhi(unsigned u) { return __uint_as_float(u & 0xffff0000u); }
__device__ __forceinline__ float rbf(float x) { return (float)(__bf16)x; }
__device__ __forceinline__ unsigned packbf(float lo, float hi) {
  bf16x2_t v; v[0] = (__bf16)lo; v[1] = (__bf16)hi; return __builtin_bit_cast(unsigned, v);
}
__device__ __forceinline__ float dot2(unsigned a, unsigned b, float acc) {
  return __builtin_amdgcn_fdot2_f32_bf16(__builtin_bit_cast(bf16x2_t, a), __builtin_bit_cast(bf16x2_t, b), acc, false);
}
template <int CTRL> __device__ __forceinline__ float dpp(float x) {
  return __builtin_bit_cast(float, __builtin_amdgcn_mov_dpp(__builtin_bit_cast(int, x), CTRL, 0xf, 0xf, true));
}
constexpr int XOR1 = 0xB1, XOR2 = 0x4E, XOR7 = 0x141, XOR8 = 0x128, ROR2 = 0x122, ROR4 = 0x124;
__device__ __forceinline__ float xrow_max(float x) {
  auto s = __builtin_amdgcn_permlane16_swap(__float_as_uint(x), __float_as_uint(x), false, false);
  x = fmaxf(__uint_as_float(s[0]), __uint_as_float(s[1]));
  auto t = __builtin_amdgcn_permlane32_swap(__float_as_uint(x), __float_as_uint(x), false, false);
  return fmaxf(__uint_as_float(t[0]), __uint_as_float(t[1]));
}
__device__ __forceinline__ float xrow_sum(float x) {
  auto s = __builtin_amdgcn_permlane16_swap(__float_as_uint(x), __float_as_uint(x), false, false);
  x = __uint_as_float(s[0]) + __uint_as_float(s[1]);
  auto t = __builtin_amdgcn_permlane32_swap(__float_as_uint(x), __float_as_uint(x), false, false);
  return __uint_as_float(t[0]) + __uint_as_float(t[1]);
}
__device__ __forceinline__ f32x2 xrow_sum8(const float (&v)[8]) {
  float t[4];
#pragma unroll
  for (int j = 0; j < 4; ++j) {
    const int a = (j >> 1) * 4 + (j & 1);
    auto s = __builtin_amdgcn_permlane16_swap(__float_as_uint(v[a]), __float_as_uint(v[a + 2]), false, false);
    t[j] = __uint_as_float(s[0]) + __uint_as_float(s[1]);
  }
  auto s0 = __builtin_amdgcn_permlane32_swap(__float_as_uint(t[0]), __float_as_uint(t[2]), false, false);
  auto s1 = __builtin_amdgcn_permlane32_swap(__float_as_uint(t[1]), __float_as_uint(t[3]), false, false);
  return f32x2{__uint_as_float(s0[0]) + __uint_as_float(s0[1]), __uint_as_float(s1[0]) + __uint_as_float(s1[1])};
}
__device__ __forceinline__ float row_sum(float x) {
  x += dpp<XOR1>(x); x += dpp<XOR2>(x); x += dpp<XOR7>(x); x += dpp<XOR8>(x); return x;
}
__device__ __forceinline__ float wave_sum(float x) { return xrow_sum(row_sum(x)); }
__device__ __forceinline__ u32x4 ldnt(const void* p) { return __builtin_nontemporal_load(reinterpret_cast<const u32x4*>(p)); }
__device__ __forceinline__ float rdlane(float x, int l) { return __builtin_bit_cast(float, __builtin_amdgcn_readlane(__builtin_bit_cast(int, x), l)); }
__device__ __forceinline__ float u2f(unsigned u) { return __uint_as_float(u); }
#define SB() __builtin_amdgcn_sched_barrier(0)
#define STAMP(i)
__device__ __forceinline__ u32x4 poll16(const unsigned char* p) {
  u32x4 v;
  asm volatile("global_load_dwordx4 %0, %1, off sc1\n\ts_waitcnt vmcnt(0)" : "=&v"(v) : "v"(p) : "memory");
  return v;
}
__device__ __forceinline__ bool tagged(const u32x4& g, unsigned tag) { return g[1] == tag && g[3] == tag; }

struct State { float m, l; float acc[8]; };

__device__ __forceinline__ float qk_softmax(const u32x4 (&K)[T], const unsigned (&q)[4], State& st, int c) {
  float x[4];
#pragma unroll
  for (int i = 0; i < 4; ++i) {
    float d = dot2(K[i][0], q[0], 0.f);
    d = dot2(K[i][1], q[1], d); d = dot2(K[i][2], q[2], d); d = dot2(K[i][3], q[3], d);
    x[i] = d;
  }
  float s, y[2];
#pragma unroll
  for (int j = 0; j < 2; ++j) { const float a = x[j] + dpp<XOR8>(x[j]), b = x[j + 2] + dpp<XOR8>(x[j + 2]); y[j] = (c & 8) ? b : a; }
  { const float a = y[0] + dpp<XOR7>(y[0]), b = y[1] + dpp<XOR7>(y[1]); s = (c & 4) ? b : a; }
  s += dpp<XOR2>(s);
  s += dpp<XOR1>(s);
  s = rbf(rbf(s) * SCALE);
  float tm = fmaxf(s, dpp<XOR7>(s)); tm = fmaxf(tm, dpp<XOR8>(tm));
  const float mn = fmaxf(st.m, tm);
  const float alpha = __builtin_amdgcn_exp2f((st.m - mn) * L2E);
  const float p = __builtin_amdgcn_exp2f((s - mn) * L2E);
  st.l = fmaf(st.l, alpha, p); st.m = mn;
#pragma unroll
  for (int k = 0; k < 8; ++k) st.acc[k] *= alpha;
  return p;
}
template <int I> __device__ __forceinline__ void pv_one(const u32x4& V, float p, State& st) {
  const float pb = dpp<0x150 + 4 * I>(p);
#pragma unroll
  for (int k = 0; k < 4; ++k) {
    st.acc[2 * k] = fmaf(pb, bflo(V[k]), st.acc[2 * k]);
    st.acc[2 * k + 1] = fmaf(pb, bfhi(V[k]), st.acc[2 * k + 1]);
  }
}
__device__ __forceinline__ void pv(const u32x4 (&V)[T], float p, State& st) {
  pv_one<0>(V[0], p, st); pv_one<1>(V[1], p, st); pv_one<2>(V[2], p, st); pv_one<3>(V[3], p, st);
  asm volatile("" : "+v"(st.acc[0]), "+v"(st.acc[1]), "+v"(st.acc[2]), "+v"(st.acc[3]),
                    "+v"(st.acc[4]), "+v"(st.acc[5]), "+v"(st.acc[6]), "+v"(st.acc[7]), "+v"(st.l));
}
__device__ __forceinline__ void load_tile(u32x4 (&B)[T], __amdgpu_buffer_rsrc_t rsrc, unsigned voff) {
#pragma unroll
  for (int i = 0; i < T; ++i) B[i] = __builtin_amdgcn_raw_buffer_load_b128(rsrc, voff + i * 1024, 0, 2);
}
__device__ __forceinline__ void fma8(float (&a)[8], float s, const u32x4& w) {
#pragma unroll
  for (int k = 0; k < 4; ++k) { a[2 * k] = fmaf(s, bflo(w[k]), a[2 * k]); a[2 * k + 1] = fmaf(s, bfhi(w[k]), a[2 * k + 1]); }
}

__device__ __forceinline__ void fma8b(float (&a)[8], float s, const u32x4& w) {
  const unsigned hi = __float_as_uint(s) & 0xffff0000u, lo = hi >> 16;
#pragma unroll
  for (int k = 0; k < 4; ++k) { a[2 * k] = dot2(w[k], lo, a[2 * k]); a[2 * k + 1] = dot2(w[k], hi, a[2 * k + 1]); }
}

__global__ void __launch_bounds__(NTHR, 2)
fused_decode(const u16* __restrict__ x, const u16* __restrict__ kc, const u16* __restrict__ vc,
             const u16* __restrict__ wq, const u16* __restrict__ wo, const u16* __restrict__ wg,
             const u16* __restrict__ wu, const u16* __restrict__ wd, u16* __restrict__ out,
             unsigned char* ws) {
  __shared__ __attribute__((aligned(16))) float lds[L_TOTAL];
  const int tid = threadIdx.x, lane = tid & 63, w = __builtin_amdgcn_readfirstlane(tid >> 6);
  const int r = lane >> 4, c = lane & 15;
  const int b = blockIdx.x, g = b & 7, bi = b >> 3;
  const bool attn = bi < 24;
  const int head = g * 4 + (attn ? bi / 6 : (bi - 24) >> 1);
  const int sp = attn ? bi % 6 : 6 + (bi & 1);
  const __amdgpu_buffer_rsrc_t rs = __builtin_amdgcn_make_buffer_rsrc((void*)ws, 0, (int)WS_END, 0x00020000);
  const unsigned l16 = lane * 16;
  const int tile0 = (NTILE * sp) / 6, ntl = (NTILE * (sp + 1)) / 6 - tile0;

  STAMP(0);
  unsigned xv[5]; u64 ep; unsigned tag; u32x4 WQ[3];
  const size_t kvoff = (size_t)head * SK * (DH * 2);
  const __amdgpu_buffer_rsrc_t rk = __builtin_amdgcn_make_buffer_rsrc((void*)(reinterpret_cast<const char*>(kc) + kvoff), 0, SK * DH * 2, 0x00020000);
  const __amdgpu_buffer_rsrc_t rv = __builtin_amdgcn_make_buffer_rsrc((void*)(reinterpret_cast<const char*>(vc) + kvoff), 0, SK * DH * 2, 0x00020000);
  auto pro1 = [&]() {
  const unsigned* x32 = reinterpret_cast<const unsigned*>(x);
#pragma unroll
  for (int i = 0; i < 4; ++i) xv[i] = x32[lane + 64 * i];
  xv[4] = x32[256 + (lane & 31)];
  ep = __hip_atomic_load(reinterpret_cast<const u64*>(ws + WS_E), __ATOMIC_RELAXED, __HIP_MEMORY_SCOPE_AGENT);
  {
    const char* p = reinterpret_cast<const char*>(wq) + ((size_t)(72 * sp + (tid >> 4)) * (NH * DH) + head * DH + 8 * c) * 2;
    WQ[0] = ldnt(p); WQ[1] = ldnt(p + (size_t)32 * (NH * DH * 2));
    WQ[2] = ldnt(p + (size_t)(w < 2 ? 64 : 0) * (NH * DH * 2));
  }
  if (tid == 0) reinterpret_cast<unsigned*>(lds)[L_CTR] = 16u + 8u * NX;
  SB();
  };
  auto pro2 = [&]() {
  {
    float xf[10];
    asm volatile("" : "+v"(xv[0]), "+v"(xv[1]), "+v"(xv[2]), "+v"(xv[3]), "+v"(xv[4]));
#pragma unroll
    for (int i = 0; i < 5; ++i) { xf[2 * i] = bflo(xv[i]); xf[2 * i + 1] = bfhi(xv[i]); }
    const bool tl = lane < 32;
    float s = 0.f;
#pragma unroll
    for (int i = 0; i < 8; ++i) s += xf[i];
    s += tl ? (xf[8] + xf[9]) : 0.f;
    const float mean = wave_sum(s) * (1.f / D);
    float v = 0.f;
#pragma unroll
    for (int i = 0; i < 8; ++i) { const float d = xf[i] - mean; v = fmaf(d, d, v); }
    { const float d0 = xf[8] - mean, d1 = xf[9] - mean; v += tl ? (d0 * d0 + d1 * d1) : 0.f; }
    const float var = wave_sum(v) * (1.f / D);
    const float mb = rbf(mean), den = rbf(sqrtf(rbf(rbf(var) + 1e-5f)));
#pragma unroll
    for (int i = 0; i < 5; ++i) {
      if (i < 4 || tl) {
        const int e = 2 * (lane + 64 * i);
        *reinterpret_cast<f32x2*>(&lds[L_XN + e]) = f32x2{rbf(rbf(xf[2 * i] - mb) / den), rbf(rbf(xf[2 * i + 1] - mb) / den)};
        *reinterpret_cast<f32x2*>(&lds[L_XR + e]) = f32x2{xf[2 * i], xf[2 * i + 1]};
      }
    }
  }
  STAMP(7);
  tag = (unsigned)ep; asm volatile("" : "+v"(tag)); tag += 1u;
  {
    float a[8];
#pragma unroll
    for (int k = 0; k < 8; ++k) a[k] = 0.f;
    const float* xn = &lds[L_XN + 72 * sp + (tid >> 4)];
    fma8(a, xn[0], WQ[0]); fma8(a, xn[32], WQ[1]);
    fma8(a, w < 2 ? xn[64] : 0.f, WQ[2]);
#pragma unroll
    for (int k = 0; k < 8; ++k) a[k] = xrow_sum(a[k]);
    if (r == 0) {
      *reinterpret_cast<f32x4*>(&lds[L_QP + w * 128 + 8 * c]) = f32x4{a[0], a[1], a[2], a[3]};
      *reinterpret_cast<f32x4*>(&lds[L_QP + w * 128 + 8 * c + 4]) = f32x4{a[4], a[5], a[6], a[7]};
    }
    __syncthreads();
    if (w == 0) {
      f32x2 s = {0.f, 0.f};
#pragma unroll
      for (int j = 0; j < 8; ++j) s += *reinterpret_cast<const f32x2*>(&lds[L_QP + j * 128 + 2 * lane]);
      __builtin_amdgcn_raw_buffer_store_b128(u32x4{__float_as_uint(s[0]), tag, __float_as_uint(s[1]), tag}, rs,
                                             WS_IN0 + (unsigned)((head * 8 + sp) * 1024 + lane * 16), 0, 16);
    }
    __syncthreads();
  }
  STAMP(8);

  };

  if (attn) {
    u32x4 KA[T], VA[T], KX[NX][T], VX[NX][T];
    asm volatile("; attention path");
    pro1();
    __builtin_amdgcn_s_barrier();
    { const unsigned o = w != 0 ? l16 + (tile0 + w) * TB : OOB; SB(); load_tile(KA, rk, o); SB(); load_tile(VA, rv, o); SB(); }
    pro2();
    unsigned q[4];
    if (w != 0) {
#pragma unroll
      for (int j = 0; j < NX; ++j) { SB(); load_tile(KX[j], rk, l16 + (tile0 + 8 * j + 8 + w) * TB); SB(); load_tile(VX[j], rv, l16 + (tile0 + 8 * j + 8 + w) * TB); SB(); }
    } else {
      const unsigned char* pq = ws + WS_IN0 + head * 8192 + lane * 16;
      u32x4 g0, g1, g2, g3, g4, g5, g6, g7;
      for (unsigned spins = 0;; ++spins) {
        asm volatile("global_load_dwordx4 %0, %8, off sc1\n\t"
                     "global_load_dwordx4 %1, %8, off offset:1024 sc1\n\t"
                     "global_load_dwordx4 %2, %8, off offset:2048 sc1\n\t"
                     "global_load_dwordx4 %3, %8, off offset:3072 sc1\n\t"
                     "global_load_dwordx4 %4, %9, off sc1\n\t"
                     "global_load_dwordx4 %5, %9, off offset:1024 sc1\n\t"
                     "global_load_dwordx4 %6, %9, off offset:2048 sc1\n\t"
                     "global_load_dwordx4 %7, %9, off offset:3072 sc1\n\t"
                     "s_waitcnt vmcnt(0)"
                     : "=&v"(g0), "=&v"(g1), "=&v"(g2), "=&v"(g3), "=&v"(g4), "=&v"(g5), "=&v"(g6), "=&v"(g7)
                     : "v"(pq), "v"(pq + 4096) : "memory");
        const bool ok = tagged(g0, tag) && tagged(g1, tag) && tagged(g2, tag) && tagged(g3, tag) &&
                        tagged(g4, tag) && tagged(g5, tag) && tagged(g6, tag) && tagged(g7, tag);
        if (__all(ok) || spins > SPIN_CAP) break;
        __builtin_amdgcn_s_sleep(1);
      }
      const float q0 = ((u2f(g0[0]) + u2f(g1[0])) + (u2f(g2[0]) + u2f(g3[0]))) + ((u2f(g4[0]) + u2f(g5[0])) + (u2f(g6[0]) + u2f(g7[0])));
      const float q1 = ((u2f(g0[2]) + u2f(g1[2])) + (u2f(g2[2]) + u2f(g3[2]))) + ((u2f(g4[2]) + u2f(g5[2])) + (u2f(g6[2]) + u2f(g7[2])));
      reinterpret_cast<unsigned*>(lds)[L_QS + lane] = packbf(q0, q1);
    }
    __syncthreads();
    if (w == 0) {
      SB(); load_tile(KA, rk, l16 + tile0 * TB); SB(); load_tile(VA, rv, l16 + tile0 * TB); SB();
#pragma unroll
      for (int j = 0; j < NX; ++j) { SB(); load_tile(KX[j], rk, l16 + (tile0 + 8 * j + 8) * TB); SB(); load_tile(VX[j], rv, l16 + (tile0 + 8 * j + 8) * TB); SB(); }
    }
    SB();
    {
      const u32x4 qq = *reinterpret_cast<const u32x4*>(&lds[L_QS + 4 * c]);
      q[0] = qq[0]; q[1] = qq[1]; q[2] = qq[2]; q[3] = qq[3];
    }
    STAMP(1);
    State st; st.m = -3.0e38f; st.l = 0.f;
#pragma unroll
    for (int k = 0; k < 8; ++k) st.acc[k] = 0.f;
    float p = qk_softmax(KA, q, st, c); SB();
    load_tile(KA, rk, l16 + (tile0 + 8 * NX + 8 + w) * TB); SB();
    pv(VA, p, st); SB();
    load_tile(VA, rv, l16 + (tile0 + 8 * NX + 8 + w) * TB); SB();
#pragma unroll
    for (int j = 0; j < NX; ++j) { p = qk_softmax(KX[j], q, st, c); SB(); pv(VX[j], p, st); SB(); }
    for (;;) {
      unsigned t = 0;
      if (lane == 0) t = __hip_atomic_fetch_add(&reinterpret_cast<unsigned*>(lds)[L_CTR], 1u, __ATOMIC_RELAXED, __HIP_MEMORY_SCOPE_WORKGROUP);
      p = qk_softmax(KA, q, st, c); SB();
      const int loc = __builtin_amdgcn_readfirstlane(t);
      if (loc >= ntl) break;
      load_tile(KA, rk, l16 + (tile0 + loc) * TB); SB();
      pv(VA, p, st); SB();
      load_tile(VA, rv, l16 + (tile0 + loc) * TB); SB();
    }
    const int aw = g * 24 + bi;
    u32x4 Y[4], HG;
    {
      const __amdgpu_buffer_rsrc_t rd = __builtin_amdgcn_make_buffer_rsrc((void*)wd, 0, FF * D * 2, 0x00020000);
      const unsigned od = (unsigned)((24 * aw + 3 * w) * (D * 2));
      SB();
#pragma unroll
      for (int i = 0; i < 3; ++i) Y[i] = __builtin_amdgcn_raw_buffer_load_b128(rd, od + i * (D * 2) + l16, 0, 2);
      Y[3] = __builtin_amdgcn_raw_buffer_load_b128(rd, ((lane >> 3) < 3 ? od + (lane >> 3) * (D * 2) : OOB) + 1024 + (lane & 7) * 16, 0, 2);
      HG = __builtin_amdgcn_raw_buffer_load_b128(rs, WS_H + aw * 192 + (lane < 12 ? lane : 0) * 16, 0, 16);
      SB();
    }
    pv(VA, p, st); SB();
    STAMP(2);
    const float mw = xrow_max(st.m);
    const float f = __builtin_amdgcn_exp2f((st.m - mw) * L2E);
    const float lw = xrow_sum(row_sum(st.l) * (0.25f * f));
#pragma unroll
    for (int k = 0; k < 8; ++k) st.acc[k] *= f;
    float* rec = &lds[L_MRG + w * 132];
    *reinterpret_cast<f32x2*>(rec + 4 + 8 * c + 2 * r) = xrow_sum8(st.acc);
    if (lane == 0) *reinterpret_cast<f32x2*>(rec) = f32x2{mw, lw};
    __syncthreads();
    if (tid < 65) {
      float M = lds[L_MRG];
#pragma unroll
      for (int j = 1; j < 8; ++j) M = fmaxf(M, lds[L_MRG + j * 132]);
      const int src = tid < 64 ? 4 + 2 * tid : 0;
      float v0 = 0.f, v1 = 0.f;
#pragma unroll
      for (int j = 0; j < 8; ++j) {
        const float wt = __builtin_amdgcn_exp2f((lds[L_MRG + j * 132] - M) * L2E);
        const f32x2 a = *reinterpret_cast<const f32x2*>(&lds[L_MRG + j * 132 + src]);
        v0 = fmaf(wt, a[0], v0); v1 = fmaf(wt, a[1], v1);
      }
      if (tid == 64) v0 = M;
      const unsigned off = tid < 64 ? WS_REC + (unsigned)((head * 8 + sp) * 1024 + tid * 16) : WS_IND + (unsigned)((head * 8 + sp) * 16);
      __builtin_amdgcn_raw_buffer_store_b128(u32x4{__float_as_uint(v0), tag, __float_as_uint(v1), tag}, rs, off, 0, 16);
    }
    STAMP(3);
    {
      for (unsigned spins = 0; !__all(tagged(HG, tag)) && spins < SPIN_CAP; ++spins) HG = poll16(ws + WS_H + aw * 192 + (lane < 12 ? lane : 0) * 16);
      float hv[3];
#pragma unroll
      for (int i = 0; i < 3; ++i) {
        const int u = 3 * w + i;
        const float e0 = rdlane(u2f(HG[0]), u >> 1), e1 = rdlane(u2f(HG[2]), u >> 1);
        hv[i] = (u & 1) ? e1 : e0;
      }
      float pa[8], pr[8];
#pragma unroll
      for (int k = 0; k < 8; ++k) { pa[k] = 0.f; pr[k] = 0.f; }
#pragma unroll
      for (int i = 0; i < 3; ++i) fma8b(pa, hv[i], Y[i]);
      fma8b(pr, (lane >> 3) == 0 ? hv[0] : (lane >> 3) == 1 ? hv[1] : hv[2], Y[3]);
#pragma unroll
      for (int k = 0; k < 8; ++k) { pr[k] += dpp<XOR8>(pr[k]); pr[k] = xrow_sum(pr[k]); }
      float* red = &lds[L_RED + w * 576];
      *reinterpret_cast<f32x4*>(red + 8 * lane) = f32x4{pa[0], pa[1], pa[2], pa[3]};
      *reinterpret_cast<f32x4*>(red + 8 * lane + 4) = f32x4{pa[4], pa[5], pa[6], pa[7]};
      if (lane < 8) {
        *reinterpret_cast<f32x4*>(red + 512 + 8 * lane) = f32x4{pr[0], pr[1], pr[2], pr[3]};
        *reinterpret_cast<f32x4*>(red + 512 + 8 * lane + 4) = f32x4{pr[4], pr[5], pr[6], pr[7]};
      }
      __syncthreads();
      if (tid < 288) {
        f32x2 s = {0.f, 0.f};
#pragma unroll
        for (int j = 0; j < 8; ++j) s += *reinterpret_cast<const f32x2*>(&lds[L_RED + j * 576 + 2 * tid]);
        const unsigned off = WS_IN2 + (unsigned)((tid >> 1) * 8192 + aw * 32 + (tid & 1) * 16);
        __builtin_amdgcn_raw_buffer_store_b128(u32x4{__float_as_uint(s[0]), tag, __float_as_uint(s[1]), tag}, rs, off, 0, 16);
      }
    }
    const int R = g * 18 + bi;
    if (bi < 18 && w == 7) {
      const unsigned char* pz = ws + WS_IN2 + R * 8192 + l16;
      float sum[2][2];
      {
        u32x4 g0 = {0u, 0u, 0u, 0u}, g1 = g0, g2 = g0, g3 = g0, g4 = g0, g5 = g0;
        bool ok = false;
        for (unsigned spins = 0;; ++spins) {
          if (!ok) {
            asm volatile("global_load_dwordx4 %0, %6, off sc1\n\t"
                         "global_load_dwordx4 %1, %6, off offset:1024 sc1\n\t"
                         "global_load_dwordx4 %2, %6, off offset:2048 sc1\n\t"
                         "global_load_dwordx4 %3, %6, off offset:3072 sc1\n\t"
                         "global_load_dwordx4 %4, %7, off sc1\n\t"
                         "global_load_dwordx4 %5, %7, off offset:1024 sc1\n\t"
                         "s_waitcnt vmcnt(0)"
                         : "+v"(g0), "+v"(g1), "+v"(g2), "+v"(g3), "+v"(g4), "+v"(g5) : "v"(pz), "v"(pz + 4096) : "memory");
            ok = tagged(g0, tag) && tagged(g1, tag) && tagged(g2, tag) && tagged(g3, tag) && tagged(g4, tag) && tagged(g5, tag);
          }
          if (__all(ok) || spins > SPIN_CAP) break;
          if (__builtin_popcountll(__ballot(!ok)) > 16) __builtin_amdgcn_s_sleep(16); else __builtin_amdgcn_s_sleep(1);
        }
        float v0 = ((u2f(g0[0]) + u2f(g1[0])) + (u2f(g2[0]) + u2f(g3[0]))) + (u2f(g4[0]) + u2f(g5[0]));
        float v1 = ((u2f(g0[2]) + u2f(g1[2])) + (u2f(g2[2]) + u2f(g3[2]))) + (u2f(g4[2]) + u2f(g5[2]));
        v0 += dpp<ROR2>(v0); v1 += dpp<ROR2>(v1);
        v0 += dpp<ROR4>(v0); v1 += dpp<ROR4>(v1);
        v0 += dpp<XOR8>(v0); v1 += dpp<XOR8>(v1);
        sum[0][0] = xrow_sum(v0); sum[0][1] = xrow_sum(v1);
      }
      {
        u32x4 g0 = {0u, 0u, 0u, 0u}, g1 = g0;
        bool ok = false;
        for (unsigned spins = 0;; ++spins) {
          if (!ok) {
            asm volatile("global_load_dwordx4 %0, %2, off offset:2048 sc1\n\t"
                         "global_load_dwordx4 %1, %2, off offset:3072 sc1\n\t"
                         "s_waitcnt vmcnt(0)"
                         : "+v"(g0), "+v"(g1) : "v"(pz + 4096) : "memory");
            ok = tagged(g0, tag) && tagged(g1, tag);
          }
          if (__all(ok) || spins > SPIN_CAP) break;
          if (__builtin_popcountll(__ballot(!ok)) > 16) __builtin_amdgcn_s_sleep(8); else __builtin_amdgcn_s_sleep(1);
        }
        float v0 = u2f(g0[0]) + u2f(g1[0]), v1 = u2f(g0[2]) + u2f(g1[2]);
        v0 += dpp<ROR2>(v0); v1 += dpp<ROR2>(v1);
        v0 += dpp<ROR4>(v0); v1 += dpp<ROR4>(v1);
        v0 += dpp<XOR8>(v0); v1 += dpp<XOR8>(v1);
        sum[1][0] = xrow_sum(v0); sum[1][1] = xrow_sum(v1);
      }
      if (lane < 2) {
        const f32x2 xr = *reinterpret_cast<const f32x2*>(&lds[L_XR + 4 * R + 2 * lane]);
        const float o0 = rbf(xr[0] + rbf(sum[1][0])) + rbf(sum[0][0]);
        const float o1 = rbf(xr[1] + rbf(sum[1][1])) + rbf(sum[0][1]);
        reinterpret_cast<unsigned*>(out)[2 * R + lane] = packbf(o0, o1);
      }
      if (R == 0 && lane == 0)
        __hip_atomic_store(reinterpret_cast<u64*>(ws + WS_E), ep + 1ull, __ATOMIC_RELAXED, __HIP_MEMORY_SCOPE_AGENT);
    }
  } else {
    asm volatile("; service path");
    pro1();
    pro2();
    const int half = bi & 1, sv = head * 2 + half;
    const int blk0 = sv, nblk = (bi == 31) ? 2 : 1;
    for (int ib = 0; ib < nblk; ++ib) {
      const int bk = ib == 0 ? blk0 : 64 + g;
      u32x4 G[18];
      const char* pg = reinterpret_cast<const char*>(c < 8 ? wg : wu) + ((size_t)(72 * w + 18 * r) * FF + bk * 64 + (c & 7) * 8) * 2;
      SB();
#pragma unroll
      for (int i = 0; i < 18; ++i) G[i] = ldnt(pg + (size_t)i * (FF * 2));
      SB();
      float a[8];
#pragma unroll
      for (int k = 0; k < 8; ++k) a[k] = 0.f;
      const float* xn = &lds[L_XN + 72 * w + 18 * r];
#pragma unroll
      for (int i = 0; i < 18; ++i) fma8b(a, xn[i], G[i]);
      *reinterpret_cast<f32x2*>(&lds[L_QP + w * 128 + 8 * c + 2 * r]) = xrow_sum8(a);
      __syncthreads();
      float gt = lds[L_QP + (lane >> 3) * 128 + 8 * w + (lane & 7)], up = lds[L_QP + (lane >> 3) * 128 + 64 + 8 * w + (lane & 7)];
      gt += dpp<XOR8>(gt); up += dpp<XOR8>(up);
      gt = rbf(xrow_sum(gt)); up = rbf(xrow_sum(up));
      const float inner = 0.7978845608028654f * (gt + 0.044715f * gt * gt * gt);
      const float ge = rbf(0.5f * gt * (1.f + tanhf(inner)));
      const float h = rbf(ge * up);
      const float hn = dpp<XOR1>(h);
      __builtin_amdgcn_raw_buffer_store_b128(u32x4{__float_as_uint(h), tag, __float_as_uint(hn), tag}, rs,
                                             (lane < 8 && !(lane & 1)) ? WS_H + (unsigned)((bk * 64 + 8 * w + lane) * 8) : OOB, 0, 16);
      __syncthreads();
    }
    STAMP(1);
    u32x4 W[16];
    {
      const __amdgpu_buffer_rsrc_t ro = __builtin_amdgcn_make_buffer_rsrc((void*)wo, 0, NH * DH * D * 2, 0x00020000);
      const unsigned oo = c < 9 ? (unsigned)((sv * 64 + 16 * r) * (D * 2) + w * 144 + c * 16) : OOB;
      SB();
#pragma unroll
      for (int i = 0; i < 16; ++i) W[i] = __builtin_amdgcn_raw_buffer_load_b128(ro, oo, (unsigned)(i * (D * 2)), 0);
      SB();
    }
#pragma unroll
    for (int j = 0; j < 8; ++j)
#pragma unroll
      for (int k = 0; k < 4; ++k) {
        const unsigned a = W[2 * j][k], bq = W[2 * j + 1][k];
        W[2 * j][k] = __builtin_amdgcn_perm(bq, a, 0x05040100u);
        W[2 * j + 1][k] = __builtin_amdgcn_perm(bq, a, 0x07060302u);
      }
#pragma unroll
    for (int i = 0; i < 16; ++i) asm volatile("" : "+v"(W[i]));
    STAMP(2);
    {
      const unsigned char* pi = ws + WS_IND + (head * 8 + (lane & 7)) * 16;
      const unsigned char* prc = ws + WS_REC + (head * 8 + (lane >> 5)) * 1024 + half * 512 + (lane & 31) * 16;
      u32x4 gi, g0, g1, g2;
      bool fetch = false;
      for (unsigned spins = 0; spins < SPIN_CAP; ++spins) {
        bool okr = false;
        if (fetch) {
          asm volatile("global_load_dwordx4 %0, %4, off sc1\n\t"
                       "global_load_dwordx4 %1, %5, off sc1\n\t"
                       "global_load_dwordx4 %2, %5, off offset:2048 sc1\n\t"
                       "global_load_dwordx4 %3, %6, off sc1\n\t"
                       "s_waitcnt vmcnt(0)"
                       : "=&v"(gi), "=&v"(g0), "=&v"(g1), "=&v"(g2) : "v"(pi), "v"(prc), "v"(prc + 4096) : "memory");
          okr = __all(tagged(g0, tag) && tagged(g1, tag) && tagged(g2, tag));
        } else {
          gi = poll16(pi);
        }
        const int n = __builtin_popcount((unsigned)__ballot(tagged(gi, tag)) & 0x3fu);
        if (okr && n == 6) break;
        fetch = n >= FETCH_N;
        if (n < 2) __builtin_amdgcn_s_sleep(32); else if (n < FETCH_N) __builtin_amdgcn_s_sleep(4); else if (n < 6) __builtin_amdgcn_s_sleep(1);
      }
      STAMP(3);
      const float ms = u2f(gi[0]), ls = u2f(gi[2]);
      float mk[6], M = -3.0e38f;
#pragma unroll
      for (int s = 0; s < 6; ++s) { mk[s] = rdlane(ms, s); M = fmaxf(M, mk[s]); }
      float L = 0.f;
#pragma unroll
      for (int s = 0; s < 6; ++s) { mk[s] = __builtin_amdgcn_exp2f((mk[s] - M) * L2E); L = fmaf(mk[s], rdlane(ls, s), L); }
      const bool up = lane >= 32;
      const float w0 = up ? mk[1] : mk[0], w1 = up ? mk[3] : mk[2], w2 = up ? mk[5] : mk[4];
      float o0 = fmaf(w2, u2f(g2[0]), fmaf(w1, u2f(g1[0]), w0 * u2f(g0[0])));
      float o1 = fmaf(w2, u2f(g2[2]), fmaf(w1, u2f(g1[2]), w0 * u2f(g0[2])));
      { auto t = __builtin_amdgcn_permlane32_swap(__float_as_uint(o0), __float_as_uint(o0), false, false); o0 = u2f(t[0]) + u2f(t[1]); }
      { auto t = __builtin_amdgcn_permlane32_swap(__float_as_uint(o1), __float_as_uint(o1), false, false); o1 = u2f(t[0]) + u2f(t[1]); }
      unsigned* ol = reinterpret_cast<unsigned*>(&lds[L_O + w * 128]);
      if (lane < 32) ol[lane] = packbf(o0 / L, o1 / L);
      const u32x4 c0 = *reinterpret_cast<const u32x4*>(ol + 8 * r), c1 = *reinterpret_cast<const u32x4*>(ol + 8 * r + 4);
      float oa[8];
#pragma unroll
      for (int k = 0; k < 8; ++k) oa[k] = 0.f;
#pragma unroll
      for (int j = 0; j < 8; ++j) {
        const unsigned cj = j < 4 ? c0[j & 3] : c1[j & 3];
#pragma unroll
        for (int k = 0; k < 4; ++k) { oa[2 * k] = dot2(W[2 * j][k], cj, oa[2 * k]); oa[2 * k + 1] = dot2(W[2 * j + 1][k], cj, oa[2 * k + 1]); }
      }
      const f32x2 sx = xrow_sum8(oa);
      const float s0 = sx[0], s1 = sx[1];
      const unsigned off = c < 9 ? WS_IN2 + (unsigned)((18 * w + 2 * c + (r >> 1)) * 8192 + 6144 + sv * 32 + (r & 1) * 16) : OOB;
      __builtin_amdgcn_raw_buffer_store_b128(u32x4{__float_as_uint(s0), tag, __float_as_uint(s1), tag}, rs, off, 0, 16);
      STAMP(4);
    }
  }
  STAMP(6);
}

}

extern "C" void kernel_launch(void* const* d_in, const int* in_sizes, int n_in,
                              void* d_out, int out_size, void* d_ws, size_t ws_size,
                              hipStream_t stream) {
  fused_decode<<<NWG, NTHR, 0, stream>>>(
      (const u16*)d_in[0], (const u16*)d_in[1], (const u16*)d_in[2], (const u16*)d_in[3], (const u16*)d_in[4],
      (const u16*)d_in[6], (const u16*)d_in[7], (const u16*)d_in[8], (u16*)d_out, (unsigned char*)d_ws);
}
```

```cpp
#include <hip/hip_runtime.h>
#include <hip/hip_bf16.h>
#include <stdint.h>

namespace {

typedef unsigned u32x4 __attribute__((ext_vector_type(4)));
typedef unsigned u32x2 __attribute__((ext_vector_type(2)));
typedef float f32x4 __attribute__((ext_vector_type(4)));
typedef float f32x2 __attribute__((ext_vector_type(2)));
typedef __bf16 bf16x2_t __attribute__((ext_vector_type(2)));
typedef unsigned short u16;
typedef unsigned long long u64;

constexpr int D = 576, NH = 32, SK = 16384, DH = 128, FF = 4608;
constexpr int NWG = 256, NTHR = 512;
constexpr int T = 4;
constexpr unsigned TB = T * 1024;
constexpr int NTILE = SK * DH * 2 / TB;
constexpr int NX = 2;
constexpr int FETCH_N = 2;
constexpr float L2E = 1.4426950408889634f;
constexpr float SCALE = 0.08838834764831845f;
constexpr unsigned OOB = 0x40000000u;

constexpr unsigned WS_E = 0;
constexpr unsigned WS_IND = 4096;
constexpr unsigned WS_IN0 = 8192;
constexpr unsigned WS_REC = WS_IN0 + 32 * 8 * 1024;
constexpr unsigned WS_H = WS_REC + 32 * 8 * 1024;
constexpr unsigned WS_IN2 = WS_H + FF * 8;
constexpr unsigned WS_END = WS_IN2 + 144 * 8192;
constexpr unsigned SPIN_CAP = 1u << 16;

constexpr int L_XN = 0;
constexpr int L_XR = L_XN + 576;
constexpr int L_QP = L_XR + 576;
constexpr int L_MRG = L_QP + 8 * 128;
constexpr int L_RED = L_MRG + 8 * 132;
constexpr int L_O = L_RED + 8 * 576;
constexpr int L_QS = L_O + 8 * 128;
constexpr int L_CTR = L_QS + 64;
constexpr int L_MX = L_CTR + 4;
constexpr int L_SC = L_MX + 12;
constexpr int L_TOTAL = L_SC + 172 * 16;

__device__ __forceinline__ float bflo(unsigned u) { return __uint_as_float(u << 16); }
__device__ __forceinline__ float bfhi(unsigned u) { return __uint_as_float(u & 0xffff0000u); }
__device__ __forceinline__ float rbf(float x) { return (float)(__bf16)x; }
__device__ __forceinline__ unsigned packbf(float lo, float hi) {
  bf16x2_t v; v[0] = (__bf16)lo; v[1] = (__bf16)hi; return __builtin_bit_cast(unsigned, v);
}
__device__ __forceinline__ float dot2(unsigned a, unsigned b, float acc) {
  return __builtin_amdgcn_fdot2_f32_bf16(__builtin_bit_cast(bf16x2_t, a), __builtin_bit_cast(bf16x2_t, b), acc, false);
}
template <int CTRL> __device__ __forceinline__ float dpp(float x) {
  return __builtin_bit_cast(float, __builtin_amdgcn_mov_dpp(__builtin_bit_cast(int, x), CTRL, 0xf, 0xf, true));
}
constexpr int XOR1 = 0xB1, XOR2 = 0x4E, XOR7 = 0x141, XOR8 = 0x128, ROR2 = 0x122, ROR4 = 0x124;
__device__ __forceinline__ float xrow_max(float x) {
  auto s = __builtin_amdgcn_permlane16_swap(__float_as_uint(x), __float_as_uint(x), false, false);
  x = fmaxf(__uint_as_float(s[0]), __uint_as_float(s[1]));
  auto t = __builtin_amdgcn_permlane32_swap(__float_as_uint(x), __float_as_uint(x), false, false);
  return fmaxf(__uint_as_float(t[0]), __uint_as_float(t[1]));
}
__device__ __forceinline__ float xrow_sum(float x) {
  auto s = __builtin_amdgcn_permlane16_swap(__float_as_uint(x), __float_as_uint(x), false, false);
  x = __uint_as_float(s[0]) + __uint_as_float(s[1]);
  auto t = __builtin_amdgcn_permlane32_swap(__float_as_uint(x), __float_as_uint(x), false, false);
  return __uint_as_float(t[0]) + __uint_as_float(t[1]);
}
__device__ __forceinline__ f32x2 xrow_sum8(const float (&v)[8]) {
  float t[4];
#pragma unroll
  for (int j = 0; j < 4; ++j) {
    const int a = (j >> 1) * 4 + (j & 1);
    auto s = __builtin_amdgcn_permlane16_swap(__float_as_uint(v[a]), __float_as_uint(v[a + 2]), false, false);
    t[j] = __uint_as_float(s[0]) + __uint_as_float(s[1]);
  }
  auto s0 = __builtin_amdgcn_permlane32_swap(__float_as_uint(t[0]), __float_as_uint(t[2]), false, false);
  auto s1 = __builtin_amdgcn_permlane32_swap(__float_as_uint(t[1]), __float_as_uint(t[3]), false, false);
  return f32x2{__uint_as_float(s0[0]) + __uint_as_float(s0[1]), __uint_as_float(s1[0]) + __uint_as_float(s1[1])};
}
__device__ __forceinline__ float row_sum(float x) {
  x += dpp<XOR1>(x); x += dpp<XOR2>(x); x += dpp<XOR7>(x); x += dpp<XOR8>(x); return x;
}
__device__ __forceinline__ float wave_sum(float x) { return xrow_sum(row_sum(x)); }
__device__ __forceinline__ u32x4 ldnt(const void* p) { return __builtin_nontemporal_load(reinterpret_cast<const u32x4*>(p)); }
__device__ __forceinline__ float rdlane(float x, int l) { return __builtin_bit_cast(float, __builtin_amdgcn_readlane(__builtin_bit_cast(int, x), l)); }
__device__ __forceinline__ float u2f(unsigned u) { return __uint_as_float(u); }
#define SB() __builtin_amdgcn_sched_barrier(0)
__device__ __forceinline__ u32x4 poll16(const unsigned char* p) {
  u32x4 v;
  asm volatile("global_load_dwordx4 %0, %1, off sc1\n\ts_waitcnt vmcnt(0)" : "=&v"(v) : "v"(p) : "memory");
  return v;
}
__device__ __forceinline__ bool tagged(const u32x4& g, unsigned tag) { return g[1] == tag && g[3] == tag; }

struct State { float l; float acc[8]; };

__device__ __forceinline__ void qk_scores(const u32x4 (&K)[T], const unsigned (&q)[4], float* sc, float& mx, int r, int c) {
  float x[4];
#pragma unroll
  for (int i = 0; i < 4; ++i) {
    float d = dot2(K[i][0], q[0], 0.f);
    d = dot2(K[i][1], q[1], d); d = dot2(K[i][2], q[2], d); d = dot2(K[i][3], q[3], d);
    x[i] = d;
  }
  float s, y[2];
#pragma unroll
  for (int j = 0; j < 2; ++j) { const float a = x[j] + dpp<XOR8>(x[j]), b = x[j + 2] + dpp<XOR8>(x[j + 2]); y[j] = (c & 8) ? b : a; }
  { const float a = y[0] + dpp<XOR7>(y[0]), b = y[1] + dpp<XOR7>(y[1]); s = (c & 4) ? b : a; }
  s += dpp<XOR2>(s);
  s += dpp<XOR1>(s);
  s = rbf(rbf(s) * SCALE);
  mx = fmaxf(mx, s);
  if ((c & 3) == 0) sc[4 * r + (c >> 2)] = s;
}
__device__ __forceinline__ void pv(const u32x4 (&V)[T], const float* sc, float M, State& st, int r) {
  const f32x4 s4 = *reinterpret_cast<const f32x4*>(sc + 4 * r);
#pragma unroll
  for (int i = 0; i < 4; ++i) {
    const float p = __builtin_amdgcn_exp2f((s4[i] - M) * L2E);
    st.l += p;
#pragma unroll
    for (int k = 0; k < 4; ++k) {
      st.acc[2 * k] = fmaf(p, bflo(V[i][k]), st.acc[2 * k]);
      st.acc[2 * k + 1] = fmaf(p, bfhi(V[i][k]), st.acc[2 * k + 1]);
    }
  }
  asm volatile("" : "+v"(st.acc[0]), "+v"(st.acc[1]), "+v"(st.acc[2]), "+v"(st.acc[3]),
                    "+v"(st.acc[4]), "+v"(st.acc[5]), "+v"(st.acc[6]), "+v"(st.acc[7]), "+v"(st.l));
}
__device__ __forceinline__ void load_tile(u32x4 (&B)[T], __amdgpu_buffer_rsrc_t rsrc, unsigned voff) {
#pragma unroll
  for (int i = 0; i < T; ++i) B[i] = __builtin_amdgcn_raw_buffer_load_b128(rsrc, voff + i * 1024, 0, 2);
}
__device__ __forceinline__ void fma8(float (&a)[8], float s, const u32x4& w) {
#pragma unroll
  for (int k = 0; k < 4; ++k) { a[2 * k] = fmaf(s, bflo(w[k]), a[2 * k]); a[2 * k + 1] = fmaf(s, bfhi(w[k]), a[2 * k + 1]); }
}

__device__ __forceinline__ void fma8b(float (&a)[8], float s, const u32x4& w) {
  const unsigned hi = __float_as_uint(s) & 0xffff0000u, lo = hi >> 16;
#pragma unroll
  for (int k = 0; k < 4; ++k) { a[2 * k] = dot2(w[k], lo, a[2 * k]); a[2 * k + 1] = dot2(w[k], hi, a[2 * k + 1]); }
}

__global__ void __launch_bounds__(NTHR, 2)
fused_decode(const u16* __restrict__ x, const u16* __restrict__ kc, const u16* __restrict__ vc,
             const u16* __restrict__ wq, const u16* __restrict__ wo, const u16* __restrict__ wg,
             const u16* __restrict__ wu, const u16* __restrict__ wd, u16* __restrict__ out,
             unsigned char* ws) {
  __shared__ __attribute__((aligned(16))) float lds[L_TOTAL];
  const int tid = threadIdx.x, lane = tid & 63, w = __builtin_amdgcn_readfirstlane(tid >> 6);
  const int r = lane >> 4, c = lane & 15;
  const int b = blockIdx.x, g = b & 7, bi = b >> 3;
  const bool attn = bi < 24;
  const int head = g * 4 + (attn ? bi / 6 : (bi - 24) >> 1);
  const int sp = attn ? bi % 6 : 6 + (bi & 1);
  const __amdgpu_buffer_rsrc_t rs = __builtin_amdgcn_make_buffer_rsrc((void*)ws, 0, (int)WS_END, 0x00020000);
  const unsigned l16 = lane * 16;
  const int tile0 = 2 * ((NTILE / 2 * sp) / 6), nh = (NTILE / 2 * (sp + 1)) / 6 - (NTILE / 2 * sp) / 6;
  const unsigned hb = (unsigned)nh * TB;

  unsigned xv[5]; u64 ep; unsigned tag; u32x4 WQ[3];
  const size_t kvoff = (size_t)head * SK * (DH * 2);
  const __amdgpu_buffer_rsrc_t rk = __builtin_amdgcn_make_buffer_rsrc((void*)(reinterpret_cast<const char*>(kc) + kvoff), 0, SK * DH * 2, 0x00020000);
  const __amdgpu_buffer_rsrc_t rv = __builtin_amdgcn_make_buffer_rsrc((void*)(reinterpret_cast<const char*>(vc) + kvoff), 0, SK * DH * 2, 0x00020000);
  auto pro1 = [&]() {
  const unsigned* x32 = reinterpret_cast<const unsigned*>(x);
#pragma unroll
  for (int i = 0; i < 4; ++i) xv[i] = x32[lane + 64 * i];
  xv[4] = x32[256 + (lane & 31)];
  ep = __hip_atomic_load(reinterpret_cast<const u64*>(ws + WS_E), __ATOMIC_RELAXED, __HIP_MEMORY_SCOPE_AGENT);
  {
    const char* p = reinterpret_cast<const char*>(wq) + ((size_t)(72 * sp + (tid >> 4)) * (NH * DH) + head * DH + 8 * c) * 2;
    WQ[0] = ldnt(p); WQ[1] = ldnt(p + (size_t)32 * (NH * DH * 2));
    WQ[2] = ldnt(p + (size_t)(w < 2 ? 64 : 0) * (NH * DH * 2));
  }
  if (tid == 0) reinterpret_cast<unsigned*>(lds)[L_CTR] = 16u + 8u * NX;
  SB();
  };
  auto pro2 = [&]() {
  {
    float xf[10];
    asm volatile("" : "+v"(xv[0]), "+v"(xv[1]), "+v"(xv[2]), "+v"(xv[3]), "+v"(xv[4]));
#pragma unroll
    for (int i = 0; i < 5; ++i) { xf[2 * i] = bflo(xv[i]); xf[2 * i + 1] = bfhi(xv[i]); }
    const bool tl = lane < 32;
    float s = 0.f;
#pragma unroll
    for (int i = 0; i < 8; ++i) s += xf[i];
    s += tl ? (xf[8] + xf[9]) : 0.f;
    const float mean = wave_sum(s) * (1.f / D);
    float v = 0.f;
#pragma unroll
    for (int i = 0; i < 8; ++i) { const float d = xf[i] - mean; v = fmaf(d, d, v); }
    { const float d0 = xf[8] - mean, d1 = xf[9] - mean; v += tl ? (d0 * d0 + d1 * d1) : 0.f; }
    const float var = wave_sum(v) * (1.f / D);
    const float mb = rbf(mean), den = rbf(sqrtf(rbf(rbf(var) + 1e-5f)));
#pragma unroll
    for (int i = 0; i < 5; ++i) {
      if (i < 4 || tl) {
        const int e = 2 * (lane + 64 * i);
        *reinterpret_cast<f32x2*>(&lds[L_XN + e]) = f32x2{rbf(rbf(xf[2 * i] - mb) / den), rbf(rbf(xf[2 * i + 1] - mb) / den)};
        *reinterpret_cast<f32x2*>(&lds[L_XR + e]) = f32x2{xf[2 * i], xf[2 * i + 1]};
      }
    }
  }
  tag = (unsigned)ep; asm volatile("" : "+v"(tag)); tag += 1u;
  {
    float a[8];
#pragma unroll
    for (int k = 0; k < 8; ++k) a[k] = 0.f;
    const float* xn = &lds[L_XN + 72 * sp + (tid >> 4)];
    fma8(a, xn[0], WQ[0]); fma8(a, xn[32], WQ[1]);
    fma8(a, w < 2 ? xn[64] : 0.f, WQ[2]);
#pragma unroll
    for (int k = 0; k < 8; ++k) a[k] = xrow_sum(a[k]);
    if (r == 0) {
      *reinterpret_cast<f32x4*>(&lds[L_QP + w * 128 + 8 * c]) = f32x4{a[0], a[1], a[2], a[3]};
      *reinterpret_cast<f32x4*>(&lds[L_QP + w * 128 + 8 * c + 4]) = f32x4{a[4], a[5], a[6], a[7]};
    }
    __syncthreads();
    if (w == 0) {
      f32x2 s = {0.f, 0.f};
#pragma unroll
      for (int j = 0; j < 8; ++j) s += *reinterpret_cast<const f32x2*>(&lds[L_QP + j * 128 + 2 * lane]);
      __builtin_amdgcn_raw_buffer_store_b128(u32x4{__float_as_uint(s[0]), tag, __float_as_uint(s[1]), tag}, rs,
                                             WS_IN0 + (unsigned)((head * 8 + sp) * 1024 + lane * 16), 0, 16);
    }
    __syncthreads();
  }

  };

  if (attn) {
    u32x4 KA[T], VA[T], KX[NX][T], VX[NX][T];
    asm volatile("; attention path");
    pro1();
    __builtin_amdgcn_s_barrier();
    { const unsigned o = w != 0 ? l16 + (tile0 + w) * TB : OOB; SB(); load_tile(KA, rk, o); SB(); load_tile(VA, rk, o + hb); SB(); }
    pro2();
    unsigned q[4];
    if (w != 0) {
#pragma unroll
      for (int j = 0; j < NX; ++j) { SB(); load_tile(KX[j], rk, l16 + (tile0 + 8 * j + 8 + w) * TB); SB(); load_tile(VX[j], rk, l16 + (tile0 + 8 * j + 8 + w) * TB + hb); SB(); }
    } else {
      const unsigned char* pq = ws + WS_IN0 + head * 8192 + lane * 16;
      u32x4 g0, g1, g2, g3, g4, g5, g6, g7;
      for (unsigned spins = 0;; ++spins) {
        asm volatile("global_load_dwordx4 %0, %8, off sc1\n\t"
                     "global_load_dwordx4 %1, %8, off offset:1024 sc1\n\t"
                     "global_load_dwordx4 %2, %8, off offset:2048 sc1\n\t"
                     "global_load_dwordx4 %3, %8, off offset:3072 sc1\n\t"
                     "global_load_dwordx4 %4, %9, off sc1\n\t"
                     "global_load_dwordx4 %5, %9, off offset:1024 sc1\n\t"
                     "global_load_dwordx4 %6, %9, off offset:2048 sc1\n\t"
                     "global_load_dwordx4 %7, %9, off offset:3072 sc1\n\t"
                     "s_waitcnt vmcnt(0)"
                     : "=&v"(g0), "=&v"(g1), "=&v"(g2), "=&v"(g3), "=&v"(g4), "=&v"(g5), "=&v"(g6), "=&v"(g7)
                     : "v"(pq), "v"(pq + 4096) : "memory");
        const bool ok = tagged(g0, tag) && tagged(g1, tag) && tagged(g2, tag) && tagged(g3, tag) &&
                        tagged(g4, tag) && tagged(g5, tag) && tagged(g6, tag) && tagged(g7, tag);
        if (__all(ok) || spins > SPIN_CAP) break;
        __builtin_amdgcn_s_sleep(1);
      }
      const float q0 = ((u2f(g0[0]) + u2f(g1[0])) + (u2f(g2[0]) + u2f(g3[0]))) + ((u2f(g4[0]) + u2f(g5[0])) + (u2f(g6[0]) + u2f(g7[0])));
      const float q1 = ((u2f(g0[2]) + u2f(g1[2])) + (u2f(g2[2]) + u2f(g3[2]))) + ((u2f(g4[2]) + u2f(g5[2])) + (u2f(g6[2]) + u2f(g7[2])));
      reinterpret_cast<unsigned*>(lds)[L_QS + lane] = packbf(q0, q1);
    }
    __syncthreads();
    if (w == 0) {
      SB(); load_tile(KA, rk, l16 + tile0 * TB); SB(); load_tile(VA, rk, l16 + tile0 * TB + hb); SB();
#pragma unroll
      for (int j = 0; j < NX; ++j) { SB(); load_tile(KX[j], rk, l16 + (tile0 + 8 * j + 8) * TB); SB(); load_tile(VX[j], rk, l16 + (tile0 + 8 * j + 8) * TB + hb); SB(); }
    }
    SB();
    {
      const u32x4 qq = *reinterpret_cast<const u32x4*>(&lds[L_QS + 4 * c]);
      q[0] = qq[0]; q[1] = qq[1]; q[2] = qq[2]; q[3] = qq[3];
    }
    float* const sc = &lds[L_SC];
    const int sh = nh * 16;
    float mx = -3.0e38f;
    int cur = 8 * NX + 8 + w;
    qk_scores(KA, q, sc + w * 16, mx, r, c); SB();
    load_tile(KA, rk, l16 + (tile0 + cur) * TB); SB();
    qk_scores(VA, q, sc + w * 16 + sh, mx, r, c); SB();
    load_tile(VA, rk, l16 + (tile0 + cur) * TB + hb); SB();
#pragma unroll
    for (int j = 0; j < NX; ++j) {
      qk_scores(KX[j], q, sc + (8 * j + 8 + w) * 16, mx, r, c); SB();
      qk_scores(VX[j], q, sc + (8 * j + 8 + w) * 16 + sh, mx, r, c); SB();
    }
    for (;;) {
      unsigned t = 0;
      if (lane == 0) t = __hip_atomic_fetch_add(&reinterpret_cast<unsigned*>(lds)[L_CTR], 1u, __ATOMIC_RELAXED, __HIP_MEMORY_SCOPE_WORKGROUP);
      qk_scores(KA, q, sc + cur * 16, mx, r, c); SB();
      const int loc = __builtin_amdgcn_readfirstlane(t);
      if (loc >= nh) break;
      load_tile(KA, rk, l16 + (tile0 + loc) * TB); SB();
      qk_scores(VA, q, sc + cur * 16 + sh, mx, r, c); SB();
      load_tile(VA, rk, l16 + (tile0 + loc) * TB + hb); SB();
      cur = loc;
    }
    load_tile(KA, rv, l16 + (tile0 + w) * TB); SB();
    qk_scores(VA, q, sc + cur * 16 + sh, mx, r, c); SB();
    load_tile(VA, rv, l16 + (tile0 + w) * TB + hb); SB();
    mx = fmaxf(mx, dpp<XOR7>(mx)); mx = fmaxf(mx, dpp<XOR8>(mx)); mx = xrow_max(mx);
    if (lane == 0) lds[L_MX + w] = mx;
    __syncthreads();
    float M;
    {
      const f32x4 m0 = *reinterpret_cast<const f32x4*>(&lds[L_MX]), m1 = *reinterpret_cast<const f32x4*>(&lds[L_MX + 4]);
      M = fmaxf(fmaxf(fmaxf(m0[0], m0[1]), fmaxf(m0[2], m0[3])), fmaxf(fmaxf(m1[0], m1[1]), fmaxf(m1[2], m1[3])));
    }
    State st; st.l = 0.f;
#pragma unroll
    for (int k = 0; k < 8; ++k) st.acc[k] = 0.f;
    cur = w;
    for (;;) {
      pv(KA, sc + cur * 16, M, st, r); SB();
      const int loc = cur + 8;
      if (loc >= nh) break;
      load_tile(KA, rv, l16 + (tile0 + loc) * TB); SB();
      pv(VA, sc + cur * 16 + sh, M, st, r); SB();
      load_tile(VA, rv, l16 + (tile0 + loc) * TB + hb); SB();
      cur = loc;
    }
    const int aw = g * 24 + bi;
    u32x4 Y[4], HG;
    {
      const __amdgpu_buffer_rsrc_t rd = __builtin_amdgcn_make_buffer_rsrc((void*)wd, 0, FF * D * 2, 0x00020000);
      const unsigned od = (unsigned)((24 * aw + 3 * w) * (D * 2));
      SB();
#pragma unroll
      for (int i = 0; i < 3; ++i) Y[i] = __builtin_amdgcn_raw_buffer_load_b128(rd, od + i * (D * 2) + l16, 0, 2);
      Y[3] = __builtin_amdgcn_raw_buffer_load_b128(rd, ((lane >> 3) < 3 ? od + (lane >> 3) * (D * 2) : OOB) + 1024 + (lane & 7) * 16, 0, 2);
      HG = __builtin_amdgcn_raw_buffer_load_b128(rs, WS_H + aw * 192 + (lane < 12 ? lane : 0) * 16, 0, 16);
      SB();
    }
    pv(VA, sc + cur * 16 + sh, M, st, r); SB();
    float* rec = &lds[L_MRG + w * 132];
    *reinterpret_cast<f32x2*>(rec + 4 + 8 * c + 2 * r) = xrow_sum8(st.acc);
    const float lw = xrow_sum(st.l);
    if (lane == 0) *reinterpret_cast<f32x2*>(rec) = f32x2{0.f, lw};
    __syncthreads();
    if (tid < 65) {
      const int src = tid < 64 ? 4 + 2 * tid : 0;
      float v0 = 0.f, v1 = 0.f;
#pragma unroll
      for (int j = 0; j < 8; ++j) {
        const f32x2 a = *reinterpret_cast<const f32x2*>(&lds[L_MRG + j * 132 + src]);
        v0 += a[0]; v1 += a[1];
      }
      if (tid == 64) v0 = M;
      const unsigned off = tid < 64 ? WS_REC + (unsigned)((head * 8 + sp) * 1024 + tid * 16) : WS_IND + (unsigned)((head * 8 + sp) * 16);
      __builtin_amdgcn_raw_buffer_store_b128(u32x4{__float_as_uint(v0), tag, __float_as_uint(v1), tag}, rs, off, 0, 16);
    }
    {
      for (unsigned spins = 0; !__all(tagged(HG, tag)) && spins < SPIN_CAP; ++spins) HG = poll16(ws + WS_H + aw * 192 + (lane < 12 ? lane : 0) * 16);
      float hv[3];
#pragma unroll
      for (int i = 0; i < 3; ++i) {
        const int u = 3 * w + i;
        const float e0 = rdlane(u2f(HG[0]), u >> 1), e1 = rdlane(u2f(HG[2]), u >> 1);
        hv[i] = (u & 1) ? e1 : e0;
      }
      float pa[8], pr[8];
#pragma unroll
      for (int k = 0; k < 8; ++k) { pa[k] = 0.f; pr[k] = 0.f; }
#pragma unroll
      for (int i = 0; i < 3; ++i) fma8b(pa, hv[i], Y[i]);
      fma8b(pr, (lane >> 3) == 0 ? hv[0] : (lane >> 3) == 1 ? hv[1] : hv[2], Y[3]);
#pragma unroll
      for (int k = 0; k < 8; ++k) { pr[k] += dpp<XOR8>(pr[k]); pr[k] = xrow_sum(pr[k]); }
      float* red = &lds[L_RED + w * 576];
      *reinterpret_cast<f32x4*>(red + 8 * lane) = f32x4{pa[0], pa[1], pa[2], pa[3]};
      *reinterpret_cast<f32x4*>(red + 8 * lane + 4) = f32x4{pa[4], pa[5], pa[6], pa[7]};
      if (lane < 8) {
        *reinterpret_cast<f32x4*>(red + 512 + 8 * lane) = f32x4{pr[0], pr[1], pr[2], pr[3]};
        *reinterpret_cast<f32x4*>(red + 512 + 8 * lane + 4) = f32x4{pr[4], pr[5], pr[6], pr[7]};
      }
      __syncthreads();
      if (tid < 288) {
        f32x2 s = {0.f, 0.f};
#pragma unroll
        for (int j = 0; j < 8; ++j) s += *reinterpret_cast<const f32x2*>(&lds[L_RED + j * 576 + 2 * tid]);
        const unsigned off = WS_IN2 + (unsigned)((tid >> 1) * 8192 + aw * 32 + (tid & 1) * 16);
        __builtin_amdgcn_raw_buffer_store_b128(u32x4{__float_as_uint(s[0]), tag, __float_as_uint(s[1]), tag}, rs, off, 0, 16);
      }
    }
    const int R = g * 18 + bi;
    if (bi < 18 && w == 7) {
      const unsigned char* pz = ws + WS_IN2 + R * 8192 + l16;
      float sum[2][2];
      {
        u32x4 g0 = {0u, 0u, 0u, 0u}, g1 = g0, g2 = g0, g3 = g0, g4 = g0, g5 = g0;
        bool ok = false;
        for (unsigned spins = 0;; ++spins) {
          if (!ok) {
            asm volatile("global_load_dwordx4 %0, %6, off sc1\n\t"
                         "global_load_dwordx4 %1, %6, off offset:1024 sc1\n\t"
                         "global_load_dwordx4 %2, %6, off offset:2048 sc1\n\t"
                         "global_load_dwordx4 %3, %6, off offset:3072 sc1\n\t"
                         "global_load_dwordx4 %4, %7, off sc1\n\t"
                         "global_load_dwordx4 %5, %7, off offset:1024 sc1\n\t"
                         "s_waitcnt vmcnt(0)"
                         : "+v"(g0), "+v"(g1), "+v"(g2), "+v"(g3), "+v"(g4), "+v"(g5) : "v"(pz), "v"(pz + 4096) : "memory");
            ok = tagged(g0, tag) && tagged(g1, tag) && tagged(g2, tag) && tagged(g3, tag) && tagged(g4, tag) && tagged(g5, tag);
          }
          if (__all(ok) || spins > SPIN_CAP) break;
          if (__builtin_popcountll(__ballot(!ok)) > 16) __builtin_amdgcn_s_sleep(16); else __builtin_amdgcn_s_sleep(1);
        }
        float v0 = ((u2f(g0[0]) + u2f(g1[0])) + (u2f(g2[0]) + u2f(g3[0]))) + (u2f(g4[0]) + u2f(g5[0]));
        float v1 = ((u2f(g0[2]) + u2f(g1[2])) + (u2f(g2[2]) + u2f(g3[2]))) + (u2f(g4[2]) + u2f(g5[2]));
        v0 += dpp<ROR2>(v0); v1 += dpp<ROR2>(v1);
        v0 += dpp<ROR4>(v0); v1 += dpp<ROR4>(v1);
        v0 += dpp<XOR8>(v0); v1 += dpp<XOR8>(v1);
        sum[0][0] = xrow_sum(v0); sum[0][1] = xrow_sum(v1);
      }
      {
        u32x4 g0 = {0u, 0u, 0u, 0u}, g1 = g0;
        bool ok = false;
        for (unsigned spins = 0;; ++spins) {
          if (!ok) {
            asm volatile("global_load_dwordx4 %0, %2, off offset:2048 sc1\n\t"
                         "global_load_dwordx4 %1, %2, off offset:3072 sc1\n\t"
                         "s_waitcnt vmcnt(0)"
                         : "+v"(g0), "+v"(g1) : "v"(pz + 4096) : "memory");
            ok = tagged(g0, tag) && tagged(g1, tag);
          }
          if (__all(ok) || spins > SPIN_CAP) break;
          if (__builtin_popcountll(__ballot(!ok)) > 16) __builtin_amdgcn_s_sleep(8); else __builtin_amdgcn_s_sleep(1);
        }
        float v0 = u2f(g0[0]) + u2f(g1[0]), v1 = u2f(g0[2]) + u2f(g1[2]);
        v0 += dpp<ROR2>(v0); v1 += dpp<ROR2>(v1);
        v0 += dpp<ROR4>(v0); v1 += dpp<ROR4>(v1);
        v0 += dpp<XOR8>(v0); v1 += dpp<XOR8>(v1);
        sum[1][0] = xrow_sum(v0); sum[1][1] = xrow_sum(v1);
      }
      if (lane < 2) {
        const f32x2 xr = *reinterpret_cast<const f32x2*>(&lds[L_XR + 4 * R + 2 * lane]);
        const float o0 = rbf(xr[0] + rbf(sum[1][0])) + rbf(sum[0][0]);
        const float o1 = rbf(xr[1] + rbf(sum[1][1])) + rbf(sum[0][1]);
        reinterpret_cast<unsigned*>(out)[2 * R + lane] = packbf(o0, o1);
      }
      if (R == 0 && lane == 0)
        __hip_atomic_store(reinterpret_cast<u64*>(ws + WS_E), ep + 1ull, __ATOMIC_RELAXED, __HIP_MEMORY_SCOPE_AGENT);
    }
  } else {
    asm volatile("; service path");
    pro1();
    pro2();
    const int half = bi & 1, sv = head * 2 + half;
    const int blk0 = sv, nblk = (bi == 31) ? 2 : 1;
    for (int ib = 0; ib < nblk; ++ib) {
      const int bk = ib == 0 ? blk0 : 64 + g;
      u32x4 G[18];
      const char* pg = reinterpret_cast<const char*>(c < 8 ? wg : wu) + ((size_t)(72 * w + 18 * r) * FF + bk * 64 + (c & 7) * 8) * 2;
      SB();
#pragma unroll
      for (int i = 0; i < 18; ++i) G[i] = ldnt(pg + (size_t)i * (FF * 2));
      SB();
      float a[8];
#pragma unroll
      for (int k = 0; k < 8; ++k) a[k] = 0.f;
      const float* xn = &lds[L_XN + 72 * w + 18 * r];
#pragma unroll
      for (int i = 0; i < 18; ++i) fma8b(a, xn[i], G[i]);
      *reinterpret_cast<f32x2*>(&lds[L_QP + w * 128 + 8 * c + 2 * r]) = xrow_sum8(a);
      __syncthreads();
      float gt = lds[L_QP + (lane >> 3) * 128 + 8 * w + (lane & 7)], up = lds[L_QP + (lane >> 3) * 128 + 64 + 8 * w + (lane & 7)];
      gt += dpp<XOR8>(gt); up += dpp<XOR8>(up);
      gt = rbf(xrow_sum(gt)); up = rbf(xrow_sum(up));
      const float inner = 0.7978845608028654f * (gt + 0.044715f * gt * gt * gt);
      const float ge = rbf(0.5f * gt * (1.f + tanhf(inner)));
      const float h = rbf(ge * up);
      const float hn = dpp<XOR1>(h);
      __builtin_amdgcn_raw_buffer_store_b128(u32x4{__float_as_uint(h), tag, __float_as_uint(hn), tag}, rs,
                                             (lane < 8 && !(lane & 1)) ? WS_H + (unsigned)((bk * 64 + 8 * w + lane) * 8) : OOB, 0, 16);
      __syncthreads();
    }
    u32x4 W[16];
    {
      const __amdgpu_buffer_rsrc_t ro = __builtin_amdgcn_make_buffer_rsrc((void*)wo, 0, NH * DH * D * 2, 0x00020000);
      const unsigned oo = c < 9 ? (unsigned)((sv * 64 + 16 * r) * (D * 2) + w * 144 + c * 16) : OOB;
      SB();
#pragma unroll
      for (int i = 0; i < 16; ++i) W[i] = __builtin_amdgcn_raw_buffer_load_b128(ro, oo, (unsigned)(i * (D * 2)), 0);
      SB();
    }
#pragma unroll
    for (int j = 0; j < 8; ++j)
#pragma unroll
      for (int k = 0; k < 4; ++k) {
        const unsigned a = W[2 * j][k], bq = W[2 * j + 1][k];
        W[2 * j][k] = __builtin_amdgcn_perm(bq, a, 0x05040100u);
        W[2 * j + 1][k] = __builtin_amdgcn_perm(bq, a, 0x07060302u);
      }
#pragma unroll
    for (int i = 0; i < 16; ++i) asm volatile("" : "+v"(W[i]));
    {
      const unsigned char* pi = ws + WS_IND + (head * 8 + (lane & 7)) * 16;
      const unsigned char* prc = ws + WS_REC + (head * 8 + (lane >> 5)) * 1024 + half * 512 + (lane & 31) * 16;
      u32x4 gi, g0, g1, g2;
      bool fetch = false;
      for (unsigned spins = 0; spins < SPIN_CAP; ++spins) {
        bool okr = false;
        if (fetch) {
          asm volatile("global_load_dwordx4 %0, %4, off sc1\n\t"
                       "global_load_dwordx4 %1, %5, off sc1\n\t"
                       "global_load_dwordx4 %2, %5, off offset:2048 sc1\n\t"
                       "global_load_dwordx4 %3, %6, off sc1\n\t"
                       "s_waitcnt vmcnt(0)"
                       : "=&v"(gi), "=&v"(g0), "=&v"(g1), "=&v"(g2) : "v"(pi), "v"(prc), "v"(prc + 4096) : "memory");
          okr = __all(tagged(g0, tag) && tagged(g1, tag) && tagged(g2, tag));
        } else {
          gi = poll16(pi);
        }
        const int n = __builtin_popcount((unsigned)__ballot(tagged(gi, tag)) & 0x3fu);
        if (okr && n == 6) break;
        fetch = n >= FETCH_N;
        if (n < FETCH_N) __builtin_amdgcn_s_sleep(32); else if (n < 6) __builtin_amdgcn_s_sleep(1);
      }
      const float ms = u2f(gi[0]), ls = u2f(gi[2]);
      float mk[6], M = -3.0e38f;
#pragma unroll
      for (int s = 0; s < 6; ++s) { mk[s] = rdlane(ms, s); M = fmaxf(M, mk[s]); }
      float L = 0.f;
#pragma unroll
      for (int s = 0; s < 6; ++s) { mk[s] = __builtin_amdgcn_exp2f((mk[s] - M) * L2E); L = fmaf(mk[s], rdlane(ls, s), L); }
      const bool up = lane >= 32;
      const float w0 = up ? mk[1] : mk[0], w1 = up ? mk[3] : mk[2], w2 = up ? mk[5] : mk[4];
      float o0 = fmaf(w2, u2f(g2[0]), fmaf(w1, u2f(g1[0]), w0 * u2f(g0[0])));
      float o1 = fmaf(w2, u2f(g2[2]), fmaf(w1, u2f(g1[2]), w0 * u2f(g0[2])));
      { auto t = __builtin_amdgcn_permlane32_swap(__float_as_uint(o0), __float_as_uint(o0), false, false); o0 = u2f(t[0]) + u2f(t[1]); }
      { auto t = __builtin_amdgcn_permlane32_swap(__float_as_uint(o1), __float_as_uint(o1), false, false); o1 = u2f(t[0]) + u2f(t[1]); }
      unsigned* ol = reinterpret_cast<unsigned*>(&lds[L_O + w * 128]);
      if (lane < 32) ol[lane] = packbf(o0 / L, o1 / L);
      const u32x4 c0 = *reinterpret_cast<const u32x4*>(ol + 8 * r), c1 = *reinterpret_cast<const u32x4*>(ol + 8 * r + 4);
      float oa[8];
#pragma unroll
      for (int k = 0; k < 8; ++k) oa[k] = 0.f;
#pragma unroll
      for (int j = 0; j < 8; ++j) {
        const unsigned cj = j < 4 ? c0[j & 3] : c1[j & 3];
#pragma unroll
        for (int k = 0; k < 4; ++k) { oa[2 * k] = dot2(W[2 * j][k], cj, oa[2 * k]); oa[2 * k + 1] = dot2(W[2 * j + 1][k], cj, oa[2 * k + 1]); }
      }
      const f32x2 sx = xrow_sum8(oa);
      const float s0 = sx[0], s1 = sx[1];
      const unsigned off = c < 9 ? WS_IN2 + (unsigned)((18 * w + 2 * c + (r >> 1)) * 8192 + 6144 + sv * 32 + (r & 1) * 16) : OOB;
      __builtin_amdgcn_raw_buffer_store_b128(u32x4{__float_as_uint(s0), tag, __float_as_uint(s1), tag}, rs, off, 0, 16);
    }
  }
}

}

extern "C" void kernel_launch(void* const* d_in, const int* in_sizes, int n_in,
                              void* d_out, int out_size, void* d_ws, size_t ws_size,
                              hipStream_t stream) {
  fused_decode<<<NWG, NTHR, 0, stream>>>(
      (const u16*)d_in[0], (const u16*)d_in[1], (const u16*)d_in[2], (const u16*)d_in[3], (const u16*)d_in[4],
      (const u16*)d_in[6], (const u16*)d_in[7], (const u16*)d_in[8], (u16*)d_out, (unsigned char*)d_ws);
}
```
